```python
import torch

def _softmax(x, axis=-1):
    return torch.softmax(x, dim=axis)
def _gelu(x):
    return torch.nn.functional.gelu(x, approximate="tanh")
def _silu(x):
    return torch.nn.functional.silu(x)
def _sigmoid(x):
    return torch.sigmoid(x)
def _layernorm(x, axis=-1, eps=1e-5):
    m = x.mean(dim=axis, keepdim=True)
    v = x.var(dim=axis, keepdim=True, unbiased=False)
    return (x - m) / torch.sqrt(v + eps)
def _scatter_add(tgt, idx, src, axis=0):
    return tgt.clone().index_add_(axis, idx, src)
def _segment_sum(src, seg, n):
    out = torch.zeros((n,) + src.shape[1:], dtype=src.dtype)
    return out.index_add_(0, seg, src)
def _pad(x, pw):
    flat = tuple(v for pair in reversed(pw) for v in pair)
    return torch.nn.functional.pad(x, flat)

kernel_name = 'concat_heads__compressed_kv_cla__mha_block_cached__decode'

def setup_inputs():

    _w = (lambda a: (lambda t: t.to(torch.bfloat16) if t.is_floating_point() else t)(torch.as_tensor(a)))
    def _A_setup():
        B, H, Sq, Sk, D, M = 2, 32, 1, 8192, 128, 512
        Dc = 4 * D
        return {
            "x": _w((lambda *s: torch.randn(*s, dtype=torch.bfloat16) * 3.2)(B, Sq, M)),
            "c_kv": _w((lambda *s: torch.randn(*s, dtype=torch.bfloat16) * 0.1)(B, Sk, Dc)),
            "w_q": _w((lambda *s: torch.randn(*s, dtype=torch.bfloat16) * 6.4)(M, H, D) * (M ** -0.5)),
            "w_uk": _w((lambda *s: torch.randn(*s, dtype=torch.bfloat16) * 6.4)(Dc, H, D) * (Dc ** -0.5)),
            "w_uv": _w((lambda *s: torch.randn(*s, dtype=torch.bfloat16) * 0.1)(Dc, H, D) * (Dc ** -0.5)),
            "w_o": _w((lambda *s: torch.randn(*s, dtype=torch.bfloat16) * 0.1)(H, D, M) * ((H * D) ** -0.5)),
        }
    _A_in = _A_setup()

    def _B_setup():
        B, Sq, D = 2, 1, 512
        H, Sk, Dh = 32, 8192, 128
        return {
            "x": _w((lambda *s: torch.randn(*s, dtype=torch.bfloat16) * 1.6)(B, Sq, D)),
            "k_cache": _w((lambda *s: torch.randn(*s, dtype=torch.bfloat16) * 1.6)(B, H, Sk, Dh)),
            "v_cache": _w((lambda *s: torch.randn(*s, dtype=torch.bfloat16) * 0.05)(B, H, Sk, Dh)),
            "wq": _w((lambda *s: torch.randn(*s, dtype=torch.bfloat16) * 1.6)(D, H * Dh) * (D ** -0.5)),
            "wo": _w((lambda *s: torch.randn(*s, dtype=torch.bfloat16) * 0.05)(H * Dh, D) * ((H * Dh) ** -0.5)),
        }
    _B_in = _B_setup()

    return {
        "A_x": _A_in["x"],
        "A_c_kv": _A_in["c_kv"],
        "A_w_q": _A_in["w_q"],
        "A_w_uk": _A_in["w_uk"],
        "A_w_uv": _A_in["w_uv"],
        "A_w_o": _A_in["w_o"],
        "B_x": _B_in["x"],
        "B_k_cache": _B_in["k_cache"],
        "B_v_cache": _B_in["v_cache"],
        "B_wq": _B_in["wq"],
        "B_wo": _B_in["wo"],
    }

def reference(A_x, A_c_kv, A_w_q, A_w_uk, A_w_uv, A_w_o, B_x, B_k_cache, B_v_cache, B_wq, B_wo):
    def _A_ref(x, c_kv, w_q, w_uk, w_uv, w_o):
        B, Sq, M = x.shape[0], x.shape[1], x.shape[2]
        Sk = c_kv.shape[1]
        H, D = w_q.shape[1], w_q.shape[2]
        q = torch.einsum("bqm,mhd->bhqd", x, w_q)
        k = torch.einsum("bkc,chd->bhkd", c_kv, w_uk)
        v = torch.einsum("bkc,chd->bhkd", c_kv, w_uv)
        scores = torch.einsum("bhqd,bhkd->bhqk", q, k) * (D ** -0.5)
        off = Sk - Sq
        qi = torch.arange(Sq)[:, None]
        kj = torch.arange(Sk)[None, :]
        cmask = kj <= (qi + off)
        scores = torch.where(cmask[None, None], scores, -1e9)
        p = _softmax(scores, axis=-1)
        ctx = torch.einsum("bhqk,bhkd->bhqd", p, v)
        return torch.einsum("bhqd,hdm->bqm", ctx, w_o)

    def _B_ref(x, k_cache, v_cache, wq, wo):
        B, Sq, D = x.shape
        H, Dh = 32, 128
        q = (lambda x, *d: x.permute(*d))((x @ wq).reshape(B, Sq, H, Dh), 0, 2, 1, 3)
        scores = torch.einsum("bhqd,bhkd->bhqk", q, k_cache) * (Dh ** -0.5)
        p = _softmax(scores, axis=-1)
        o = torch.einsum("bhqk,bhkd->bhqd", p, v_cache)
        return (lambda x, *d: x.permute(*d))(o, 0, 2, 1, 3).reshape(B, Sq, H * Dh) @ wo

    a = _A_ref(A_x, A_c_kv, A_w_q, A_w_uk, A_w_uv, A_w_o)
    b = _B_ref(B_x, B_k_cache, B_v_cache, B_wq, B_wo)
    return torch.concatenate([a, b], axis=-1)
```

```cpp
#include <hip/hip_runtime.h>
#include <stdint.h>

typedef unsigned int u32;
typedef unsigned short u16;
typedef __attribute__((ext_vector_type(4))) unsigned u32x4;
typedef __attribute__((ext_vector_type(2))) unsigned u32x2;
typedef __attribute__((ext_vector_type(4))) float f32x4;
typedef __attribute__((ext_vector_type(16))) float f32x16;
typedef __attribute__((ext_vector_type(8))) __bf16 bf16x8;
#define DI __device__ __forceinline__

#define MIMIC 0

constexpr int NB = 2, NH = 32, SK = 8192, HD = 128, DM = 512, DC = 512;
constexpr float SCALE = 0.08838834764831845f;
constexpr float LOG2E = 1.4426950408889634f;

DI f32x4 mfma16(u32x4 a, u32x4 b, f32x4 c) {
  return __builtin_amdgcn_mfma_f32_16x16x32_bf16(__builtin_bit_cast(bf16x8, a), __builtin_bit_cast(bf16x8, b), c, 0, 0, 0);
}
DI f32x16 mfma32(u32x4 a, u32x4 b, f32x16 c) {
  return __builtin_amdgcn_mfma_f32_32x32x16_bf16(__builtin_bit_cast(bf16x8, a), __builtin_bit_cast(bf16x8, b), c, 0, 0, 0);
}
DI u32 cvtpk(float lo, float hi) {
  typedef __attribute__((ext_vector_type(2))) float f32x2_; typedef __attribute__((ext_vector_type(2))) __bf16 bf16x2_;
  return __builtin_bit_cast(u32, __builtin_convertvector(f32x2_{lo, hi}, bf16x2_));
}
DI float bf16r(float x) { return __uint_as_float(cvtpk(x, x) << 16); }
DI float bf2f(u32 h) { return __uint_as_float(h << 16); }
template <int CTRL> DI u32 dppu(u32 x) { return (u32)__builtin_amdgcn_mov_dpp((int)x, CTRL, 0xf, 0xf, true); }
template <int CTRL> DI float dppf(float x) { return __uint_as_float(dppu<CTRL>(__float_as_uint(x))); }
DI u32x4 ldnt(const u32x4* p) { return __builtin_nontemporal_load(p); }
DI float wave_max(float x) {
  x = fmaxf(x, dppf<0xB1>(x)); x = fmaxf(x, dppf<0x4E>(x)); x = fmaxf(x, dppf<0x141>(x)); x = fmaxf(x, dppf<0x128>(x));
  auto s = __builtin_amdgcn_permlane16_swap(__float_as_uint(x), __float_as_uint(x), false, false);
  x = fmaxf(__uint_as_float(s[0]), __uint_as_float(s[1]));
  auto t = __builtin_amdgcn_permlane32_swap(__float_as_uint(x), __float_as_uint(x), false, false);
  return fmaxf(__uint_as_float(t[0]), __uint_as_float(t[1]));
}
DI float wave_sum(float x) {
  x += dppf<0xB1>(x); x += dppf<0x4E>(x); x += dppf<0x141>(x); x += dppf<0x128>(x);
  auto s = __builtin_amdgcn_permlane16_swap(__float_as_uint(x), __float_as_uint(x), false, false);
  x = __uint_as_float(s[0]) + __uint_as_float(s[1]);
  auto t = __builtin_amdgcn_permlane32_swap(__float_as_uint(x), __float_as_uint(x), false, false);
  return __uint_as_float(t[0]) + __uint_as_float(t[1]);
}
DI float quad_diag_sum(f32x4 d) {
  float s = dppf<0x00>(d[0]);
  s += dppf<0x55>(d[1]);
  s += dppf<0xAA>(d[2]);
  s += dppf<0xFF>(d[3]);
  return s;
}

constexpr size_t WS_QB = 0;
constexpr size_t WS_QABS = 65536;
constexpr size_t WS_PB = WS_QABS + 2 * 32 * 512 * 4;
constexpr int NWB = 28, PBREC = 132;
constexpr size_t WS_PAML = WS_PB + (size_t)64 * NWB * PBREC * 4;
constexpr int NSA = 16;
constexpr size_t WS_PAO = WS_PAML + 2 * NSA * 32 * 2 * 4;
constexpr size_t WS_Y = WS_PAO + (size_t)2 * NSA * 32 * 512 * 4;

struct Sel { u32 e[4], o[4]; };
DI Sel make_sel(int lane) {
  Sel s; const int slot = lane & 7, r0 = slot >> 1, half = slot & 1;
#pragma unroll
  for (int r = 0; r < 4; ++r) {
    s.e[r] = (r == r0) ? (half ? 0x01000c0cu : 0x0c0c0100u) : 0x0c0c0c0cu;
    s.o[r] = (r == r0) ? (half ? 0x03020c0cu : 0x0c0c0302u) : 0x0c0c0c0cu;
  }
  return s;
}
DI u32x4 place(u32 src, const u32 (&sel)[4]) {
  u32x4 a;
#pragma unroll
  for (int r = 0; r < 4; ++r) a[r] = __builtin_amdgcn_perm(0u, src, sel[r]);
  return a;
}

DI f32x4 vecmat_512x128(const u16* W, int ldw, const u32 (&xs)[8], const Sel& sel, int wave, int lane, f32x4 acc) {
  const int g = lane >> 4, n = lane & 15;
  const u16* p = W + (size_t)(64 * wave + 16 * g) * ldw + 8 * n;
  u32x4 w[16];
#pragma unroll
  for (int t = 0; t < 16; ++t) w[t] = *(const u32x4*)(p + (size_t)t * ldw);
#pragma unroll
  for (int t = 0; t < 16; ++t) acc = mfma16(place(xs[t >> 1], (t & 1) ? sel.o : sel.e), w[t], acc);
  return acc;
}

__global__ void __launch_bounds__(512) k1(const u16* Ax, const u16* Awq, const u16* Awuk, const u16* Bx, const u16* Bwq, unsigned char* ws) {
  __shared__ __attribute__((aligned(16))) float red[8 * 256];
  __shared__ __attribute__((aligned(16))) u16 qs[2 * 128];
  const int tid = threadIdx.x, lane = tid & 63, wave = __builtin_amdgcn_readfirstlane(tid >> 6);
  const int g = lane >> 4, i = lane & 15;
  const bool isA = blockIdx.x < 32; const int h = blockIdx.x & 31;
  const u16* x = isA ? Ax : Bx; const u16* W = (isA ? Awq : Bwq) + h * 128;
  const Sel sel = make_sel(lane);
  u32 xs[8];
  { const u32x4* xp = (const u32x4*)(x + (i >> 3) * 512 + 64 * wave + 16 * g); u32x4 a = xp[0], b = xp[1];
#pragma unroll
    for (int k = 0; k < 4; ++k) { xs[k] = a[k]; xs[4 + k] = b[k]; } }
  f32x4 acc = {0.f, 0.f, 0.f, 0.f};
  acc = vecmat_512x128(W, 4096, xs, sel, wave, lane, acc);
  *(f32x4*)&red[wave * 256 + (g >> 1) * 128 + 8 * i + 4 * (g & 1)] = acc;
  __syncthreads();
  if (tid < 256) {
    float s = 0.f;
#pragma unroll
    for (int w = 0; w < 8; ++w) s += red[w * 256 + tid];
    const u16 qb = (u16)(cvtpk(s, s) & 0xffff);
    qs[tid] = qb;
    if (!isA) ((u16*)(ws + WS_QB))[(tid >> 7) * 4096 + h * 128 + (tid & 127)] = qb;
  }
  if (!isA) return;
  __syncthreads();
  u32x4 qv = *(const u32x4*)&qs[((i >> 2) & 1) * 128 + 8 * (4 * g + (i & 3))];
  u32x4 z = {0u, 0u, 0u, 0u};
  const u32x4 qf0 = (i >> 3) == 0 ? qv : z, qf1 = (i >> 3) == 1 ? qv : z;
  const int k = (lane >> 2) & 3, cc = lane & 3;
  const u16* p = Awuk + (size_t)(64 * wave + k) * 4096 + h * 128 + 8 * (4 * g + cc);
  u32x4 w[16];
#pragma unroll
  for (int t = 0; t < 16; ++t) w[t] = *(const u32x4*)(p + (size_t)(4 * t) * 4096);
  float* qa = (float*)(ws + WS_QABS);
#pragma unroll
  for (int u = 0; u < 8; ++u) {
    f32x4 d = {0.f, 0.f, 0.f, 0.f};
    d = mfma16(w[2 * u], qf0, d); d = mfma16(w[2 * u + 1], qf1, d);
    const float s = quad_diag_sum(d);
    const int kk = lane >> 4, t = (lane >> 3) & 1, b = (lane >> 2) & 1;
    if ((lane & 3) == 0) qa[((size_t)b * 32 + h) * 512 + 64 * wave + 4 * (2 * u + t) + kk] = s;
  }
}

template <int NT, int NTL>
DI void b_stream(const u32x4* kp, const u32x4* vp, bool skip0, const u32x4 (&qf)[4], const u32 (&msk)[4], float& m, float& l, f32x4& acc) {
  u32x4 K[NT][4], V[NT][4];
#define B_ISSUE_K(s_, i_) { _Pragma("unroll") for (int t_ = 0; t_ < 4; ++t_) K[s_][t_] = ldnt(kp + (i_) * 256 + t_ * 64); }
#define B_ISSUE_V(s_, i_) { _Pragma("unroll") for (int t_ = 0; t_ < 4; ++t_) V[s_][t_] = ldnt(vp + (i_) * 256 + t_ * 64); }
#pragma unroll
  for (int i = 0; i < NT; ++i) {
    if (i > 0 || !skip0) { B_ISSUE_K(i, i) B_ISSUE_V(i, i) }
    __builtin_amdgcn_sched_barrier(0);
  }
#pragma unroll
  for (int i = 0; i < NTL; ++i) {
    constexpr int dummy = 0; (void)dummy;
    const int s = i % NT;
    if (i > 0 || !skip0) {
      f32x4 d = {0.f, 0.f, 0.f, 0.f};
#pragma unroll
      for (int t = 0; t < 4; ++t) d = mfma16(K[s][t], qf[t], d);
      __builtin_amdgcn_sched_barrier(0);
      if (i + NT < NTL) B_ISSUE_K(s, i + NT)
      __builtin_amdgcn_sched_barrier(0);
      float sc = quad_diag_sum(d);
      sc = sc * (SCALE * LOG2E);
      if (__any(sc > m)) {
        const float mn = fmaxf(m, wave_max(sc));
        const float al = __builtin_amdgcn_exp2f(m - mn);
        l *= al; acc *= al; m = mn;
      }
      const float p = __builtin_amdgcn_exp2f(sc - m);
      l += p;
      const u32 p2 = cvtpk(p, p);
#define B_PV(t_) { u32x4 a; const u32 pb = (u32)__builtin_amdgcn_mov_dpp((int)p2, 0x150 + 4 * (t_), 0xf, 0xf, true); \
        a[0] = pb & msk[0]; a[1] = pb & msk[1]; a[2] = pb & msk[2]; a[3] = pb & msk[3]; acc = mfma16(a, V[s][t_], acc); }
      B_PV(0) B_PV(1) B_PV(2) B_PV(3)
      __builtin_amdgcn_sched_barrier(0);
      if (i + NT < NTL) B_ISSUE_V(s, i + NT)
      __builtin_amdgcn_sched_barrier(0);
    } else {
      if (i + NT < NTL) { B_ISSUE_K(s, i + NT) B_ISSUE_V(s, i + NT) }
      __builtin_amdgcn_sched_barrier(0);
    }
  }
}

DI u32 off_b(u32 row, u32 ch) { return 2048u * (row >> 3) + 512u * (ch >> 2) + 64u * (row & 7) + 16u * ((ch & 3) ^ ((row >> 2) & 3)); }
typedef __attribute__((ext_vector_type(4))) short s16x4;
typedef __attribute__((address_space(3))) s16x4* lds_s16x4p;
DI u32x2 tr_read(const unsigned char* p) { return __builtin_bit_cast(u32x2, __builtin_amdgcn_ds_read_tr16_b64_v4i16((lds_s16x4p)p)); }

__global__ void __launch_bounds__(512, 2) k2(const u16* Ackv, const u16* Kc, const u16* Vc, unsigned char* ws) {
  extern __shared__ __attribute__((aligned(16))) unsigned char lds[];
  const int tid = threadIdx.x, lane = tid & 63, wave = __builtin_amdgcn_readfirstlane(tid >> 6);
  const int bx = blockIdx.x, x8 = bx & 7, j = bx >> 3, h = x8 + 8 * (j >> 3), role = j & 7;
  if (role < 7) {
    const int wv = role * 8 + wave;
    const int b = wv >= 28, w28 = wv - 28 * b;
    const int t0 = (w28 * 512) / 28, t1 = ((w28 + 1) * 512) / 28;
    const bool skip0 = (t1 - t0) == 18;
    const int tb = t1 - 19;
    const int g = lane >> 4, n = lane & 15;
    const size_t base = ((size_t)(b * 32 + h) * SK + (ptrdiff_t)tb * 16) * HD;
    const u32x4* kp = (const u32x4*)(Kc + base) + ((lane >> 2) & 3) * 16 + 4 * g + (lane & 3);
    const u32x4* vp = (const u32x4*)(Vc + base) + lane;
    const u32x4 qv = *(const u32x4*)((const u16*)(ws + WS_QB) + b * 4096 + h * 128 + 8 * (4 * g + (n & 3)));
    u32x4 qf[4]; const u32x4 z = {0u, 0u, 0u, 0u};
#pragma unroll
    for (int t = 0; t < 4; ++t) qf[t] = (n >> 2) == t ? qv : z;
    u32 msk[4];
#pragma unroll
    for (int r = 0; r < 4; ++r) msk[r] = (n < 8 && (n >> 1) == r) ? ((n & 1) ? 0xffff0000u : 0x0000ffffu) : 0u;
    float m = -INFINITY, l = 0.f; f32x4 acc = {0.f, 0.f, 0.f, 0.f};
    b_stream<4, 19>(kp, vp, skip0, qf, msk, m, l, acc);
    l = wave_sum(l) * 0.25f;
    float* rec = (float*)(ws + WS_PB) + ((size_t)(b * 32 + h) * NWB + w28) * PBREC;
    if (lane < 32) *(f32x4*)&rec[4 + 8 * n + 4 * g] = acc;
    if (lane == 0) { rec[0] = m; rec[1] = l; }
    return;
  }
  {
    const int b = h >> 4, sp = h & 15;
    const int kg = wave >> 2, cs = wave & 3;
    const int hh = lane & 31, hf = lane >> 5;
    unsigned char* img = lds + wave * 8192;
    float* sx = (float*)(lds + 65536);
    u32x4 qh[8];
    u32x4* qlp = (u32x4*)(lds + 131072) + cs * 512 + lane;
    {
      const float* qa = (const float*)(ws + WS_QABS) + ((size_t)b * 32 + hh) * 512 + 128 * cs + 8 * hf;
#pragma unroll
      for (int s = 0; s < 8; ++s) {
        const f32x4 a = *(const f32x4*)(qa + 16 * s), c = *(const f32x4*)(qa + 16 * s + 4);
        float v[8] = {a[0], a[1], a[2], a[3], c[0], c[1], c[2], c[3]};
        u32x4 ql;
#pragma unroll
        for (int k = 0; k < 4; ++k) {
          const u32 hi = cvtpk(v[2 * k], v[2 * k + 1]);
          const float r0 = v[2 * k] - bf2f(hi & 0xffff), r1 = v[2 * k + 1] - __uint_as_float(hi & 0xffff0000u);
          qh[s][k] = hi; ql[k] = cvtpk(r0, r1);
        }
        if (kg == 0) qlp[s * 64] = ql;
      }
      __syncthreads();
    }
    const u32x4* cp = (const u32x4*)(Ackv + ((size_t)b * SK + 512 * sp + 32 * kg + (lane >> 4)) * DC + 128 * cs) + (lane & 15);
    u32x4 st[2][8];
#define A_ISSUE(s_, it_) { _Pragma("unroll") for (int i_ = 0; i_ < 8; ++i_) st[s_][i_] = ldnt(cp + (size_t)((it_) * 64 + 4 * i_) * 64); }
    A_ISSUE(0, 0) A_ISSUE(1, 1)
    f32x16 o[4];
#pragma unroll
    for (int c = 0; c < 4; ++c)
#pragma unroll
      for (int r = 0; r < 16; ++r) o[c][r] = 0.f;
    float m = -INFINITY, l = 0.f;
#pragma unroll
    for (int it = 0; it < 8; ++it) {
      const int s = it & 1;
#pragma unroll
      for (int i = 0; i < 8; ++i) *(u32x4*)(img + off_b(4 * i + (lane >> 4), lane & 15)) = st[s][i];
      if (it + 2 < 8) A_ISSUE(s, it + 2)
      f32x16 sa;
#pragma unroll
      for (int r = 0; r < 16; ++r) sa[r] = 0.f;
#pragma unroll
      for (int ks = 0; ks < 8; ++ks) {
        const u32x4 a = *(const u32x4*)(img + off_b(hh, 2 * ks + hf));
        sa = mfma32(a, qh[ks], sa); sa = mfma32(a, qlp[ks * 64], sa);
      }
      float* sb = sx + (size_t)s * 8 * 1024;
#pragma unroll
      for (int q = 0; q < 4; ++q) *(f32x4*)&sb[wave * 1024 + q * 256 + lane * 4] = f32x4{sa[4 * q], sa[4 * q + 1], sa[4 * q + 2], sa[4 * q + 3]};
      __syncthreads();
#pragma unroll
      for (int r = 0; r < 16; ++r) sa[r] = 0.f;
#pragma unroll
      for (int w = 0; w < 4; ++w)
#pragma unroll
        for (int q = 0; q < 4; ++q) { const f32x4 v = *(const f32x4*)&sb[(kg * 4 + w) * 1024 + q * 256 + lane * 4];
          sa[4 * q] += v[0]; sa[4 * q + 1] += v[1]; sa[4 * q + 2] += v[2]; sa[4 * q + 3] += v[3]; }
      float mx = -INFINITY;
#pragma unroll
      for (int r = 0; r < 16; ++r) {
        sa[r] *= SCALE * LOG2E;
        mx = fmaxf(mx, sa[r]); }
      { auto t = __builtin_amdgcn_permlane32_swap(__float_as_uint(mx), __float_as_uint(mx), false, false);
        mx = fmaxf(__uint_as_float(t[0]), __uint_as_float(t[1])); }
      const float mn = fmaxf(m, mx), al = __builtin_amdgcn_exp2f(m - mn);
      m = mn; l *= al;
#pragma unroll
      for (int c = 0; c < 4; ++c) o[c] *= al;
      u32x4 pf[2];
#pragma unroll
      for (int r = 0; r < 16; r += 2) {
        const float p0 = __builtin_amdgcn_exp2f(sa[r] - m), p1 = __builtin_amdgcn_exp2f(sa[r + 1] - m);
        l += p0 + p1; pf[r >> 3][(r >> 1) & 3] = cvtpk(p0, p1);
      }
      const u32 blk = (lane >> 4) & 1, q4 = (lane & 15) >> 2, p4 = lane & 3;
#pragma unroll
      for (int ct = 0; ct < 4; ++ct)
#pragma unroll
        for (int ks = 0; ks < 2; ++ks) {
          const u32x2 lo = tr_read(img + off_b(16 * ks + 4 * hf + q4, 4 * ct + 2 * blk + (p4 >> 1)) + 8 * (p4 & 1));
          const u32x2 hi = tr_read(img + off_b(16 * ks + 8 + 4 * hf + q4, 4 * ct + 2 * blk + (p4 >> 1)) + 8 * (p4 & 1));
          const u32x4 a = {lo[0], lo[1], hi[0], hi[1]};
          o[ct] = mfma32(a, pf[ks], o[ct]);
        }
    }
    { auto t = __builtin_amdgcn_permlane32_swap(__float_as_uint(l), __float_as_uint(l), false, false);
      l = __uint_as_float(t[0]) + __uint_as_float(t[1]); }
    __syncthreads();
    float* ml = (float*)lds;
    float* ob = (float*)(lds + 1024);
    if (cs == 0 && lane < 32) { ml[(kg * 32 + hh) * 2] = m; ml[(kg * 32 + hh) * 2 + 1] = l; }
    if (kg == 1) {
#pragma unroll
      for (int ct = 0; ct < 4; ++ct)
#pragma unroll
        for (int r = 0; r < 16; ++r) ob[((cs * 4 + ct) * 16 + r) * 64 + lane] = o[ct][r];
    }
    __syncthreads();
    if (kg == 0) {
      const float m1 = ml[(32 + hh) * 2], l1 = ml[(32 + hh) * 2 + 1];
      const float mn = fmaxf(m, m1), a0 = __builtin_amdgcn_exp2f(m - mn), a1 = __builtin_amdgcn_exp2f(m1 - mn);
      float* po = (float*)(ws + WS_PAO) + ((size_t)(b * NSA + sp) * 32 + hh) * 512 + 128 * cs;
#pragma unroll
      for (int ct = 0; ct < 4; ++ct)
#pragma unroll
        for (int q = 0; q < 4; ++q) {
          f32x4 v;
#pragma unroll
          for (int e = 0; e < 4; ++e) v[e] = o[ct][4 * q + e] * a0 + ob[((cs * 4 + ct) * 16 + 4 * q + e) * 64 + lane] * a1;
          *(f32x4*)&po[32 * ct + 8 * q + 4 * hf] = v;
        }
      if (cs == 0 && lane < 32) { float* pm = (float*)(ws + WS_PAML) + ((size_t)(b * NSA + sp) * 32 + hh) * 2; pm[0] = mn; pm[1] = l * a0 + l1 * a1; }
    }
  }
}

DI void vecmat_128x512(const u16* W, const u16* os, float* red  , const Sel& sel, int wave, int lane, int tid, float* yout  , size_t ystride) {
  const int g = lane >> 4, n = lane & 15;
  u32x4 w[4][4];
#pragma unroll
  for (int t = 0; t < 4; ++t)
#pragma unroll
    for (int cb = 0; cb < 4; ++cb) w[t][cb] = *(const u32x4*)(W + (size_t)(16 * wave + 4 * g + t) * 512 + 128 * cb + 8 * n);
  const u32x2 ov = *(const u32x2*)&os[(n >> 3) * 128 + 16 * wave + 4 * g];
  f32x4 acc[4];
#pragma unroll
  for (int cb = 0; cb < 4; ++cb) acc[cb] = f32x4{0.f, 0.f, 0.f, 0.f};
#pragma unroll
  for (int t = 0; t < 4; ++t) {
    const u32x4 a = place(ov[t >> 1], (t & 1) ? sel.o : sel.e);
#pragma unroll
    for (int cb = 0; cb < 4; ++cb) acc[cb] = mfma16(a, w[t][cb], acc[cb]);
  }
#pragma unroll
  for (int cb = 0; cb < 4; ++cb) *(f32x4*)&red[wave * 1024 + (g >> 1) * 512 + 128 * cb + 8 * n + 4 * (g & 1)] = acc[cb];
  __syncthreads();
#pragma unroll
  for (int r = 0; r < 2; ++r) {
    float s = 0.f;
#pragma unroll
    for (int w8 = 0; w8 < 8; ++w8) s += red[w8 * 1024 + r * 512 + tid];
    yout[r * ystride + tid] = s;
  }
}

__global__ void __launch_bounds__(512) k3(const u16* Awuv, const u16* Awo, const u16* Bwo, unsigned char* ws) {
  __shared__ __attribute__((aligned(16))) float red[8 * 1024];
  __shared__ __attribute__((aligned(16))) u16 os[2 * 128];
  __shared__ __attribute__((aligned(16))) u16 chi[2 * 512], clo[2 * 512];
  const int tid = threadIdx.x, lane = tid & 63, wave = __builtin_amdgcn_readfirstlane(tid >> 6);
  const int g = lane >> 4, i = lane & 15;
  const bool isA = blockIdx.x < 32; const int h = blockIdx.x & 31;
  const Sel sel = make_sel(lane);
  float* y = (float*)(ws + WS_Y) + (size_t)(isA ? 0 : 1) * 2 * 32 * 512 + h * 512;
  if (isA) {
#pragma unroll
    for (int b = 0; b < 2; ++b) {
      const float* pm = (const float*)(ws + WS_PAML) + ((size_t)b * NSA * 32 + h) * 2;
      float M = -INFINITY;
      for (int s = 0; s < NSA; ++s) M = fmaxf(M, pm[s * 64]);
      float L = 0.f, v = 0.f;
      const float* po = (const float*)(ws + WS_PAO) + ((size_t)b * NSA * 32 + h) * 512 + tid;
      for (int s = 0; s < NSA; ++s) { const float wt = __builtin_amdgcn_exp2f(pm[s * 64] - M); L += wt * pm[s * 64 + 1]; v += wt * po[(size_t)s * 32 * 512]; }
      v /= L;
      const u32 hi = cvtpk(v, v) & 0xffff; const float r = v - bf2f(hi);
      chi[b * 512 + tid] = (u16)hi; clo[b * 512 + tid] = (u16)(cvtpk(r, r) & 0xffff);
    }
    __syncthreads();
    u32 xs[8];
    f32x4 acc = {0.f, 0.f, 0.f, 0.f};
    { const u32x4* xp = (const u32x4*)&chi[(i >> 3) * 512 + 64 * wave + 16 * g]; u32x4 a = xp[0], b = xp[1];
#pragma unroll
      for (int k = 0; k < 4; ++k) { xs[k] = a[k]; xs[4 + k] = b[k]; } }
    acc = vecmat_512x128(Awuv + h * 128, 4096, xs, sel, wave, lane, acc);
    { const u32x4* xp = (const u32x4*)&clo[(i >> 3) * 512 + 64 * wave + 16 * g]; u32x4 a = xp[0], b = xp[1];
#pragma unroll
      for (int k = 0; k < 4; ++k) { xs[k] = a[k]; xs[4 + k] = b[k]; } }
    acc = vecmat_512x128(Awuv + h * 128, 4096, xs, sel, wave, lane, acc);
    *(f32x4*)&red[wave * 256 + (g >> 1) * 128 + 8 * i + 4 * (g & 1)] = acc;
    __syncthreads();
    if (tid < 256) {
      float s = 0.f;
#pragma unroll
      for (int w = 0; w < 8; ++w) s += red[w * 256 + tid];
      os[tid] = (u16)(cvtpk(s, s) & 0xffff);
    }
    __syncthreads();
    vecmat_128x512(Awo + (size_t)h * 128 * 512, os, red, sel, wave, lane, tid, y, 32 * 512);
  } else {
    if (tid < 256) {
      const int b = tid >> 7, d = tid & 127;
      const float* rec = (const float*)(ws + WS_PB) + (size_t)(b * 32 + h) * NWB * PBREC;
      float M = -INFINITY;
      for (int s = 0; s < NWB; ++s) M = fmaxf(M, rec[s * PBREC]);
      float L = 0.f, v = 0.f;
      for (int s = 0; s < NWB; ++s) { const float wt = __builtin_amdgcn_exp2f(rec[s * PBREC] - M); L += wt * rec[s * PBREC + 1]; v += wt * rec[s * PBREC + 4 + d]; }
      v /= L;
      os[tid] = (u16)(cvtpk(v, v) & 0xffff);
    }
    __syncthreads();
    vecmat_128x512(Bwo + (size_t)h * 128 * 512, os, red, sel, wave, lane, tid, y, 32 * 512);
  }
}

__global__ void __launch_bounds__(256) k4(const unsigned char* ws, u16* out) {
  const int idx = blockIdx.x * 256 + threadIdx.x;
  const int part = idx >> 10, b = (idx >> 9) & 1, c = idx & 511;
  const float* y = (const float*)(ws + WS_Y) + ((size_t)(part * 2 + b) * 32) * 512 + c;
  float s = 0.f;
#pragma unroll
  for (int h = 0; h < 32; ++h) s += y[h * 512];
  out[b * 1024 + part * 512 + c] = (u16)(cvtpk(s, s) & 0xffff);
}

extern "C" void kernel_launch(void* const* d_in, const int* in_sizes, int n_in, void* d_out, int out_size, void* d_ws, size_t ws_size, hipStream_t stream) {
  const u16* Ax = (const u16*)d_in[0]; const u16* Ackv = (const u16*)d_in[1]; const u16* Awq = (const u16*)d_in[2];
  const u16* Awuk = (const u16*)d_in[3]; const u16* Awuv = (const u16*)d_in[4]; const u16* Awo = (const u16*)d_in[5];
  const u16* Bx = (const u16*)d_in[6]; const u16* Kc = (const u16*)d_in[7]; const u16* Vc = (const u16*)d_in[8];
  const u16* Bwq = (const u16*)d_in[9]; const u16* Bwo = (const u16*)d_in[10];
  unsigned char* ws = (unsigned char*)d_ws;
  constexpr int LDS2 = 163840;
  (void)hipFuncSetAttribute((const void*)k2, hipFuncAttributeMaxDynamicSharedMemorySize, LDS2);
  k1<<<64, 512, 0, stream>>>(Ax, Awq, Awuk, Bx, Bwq, ws);
  k2<<<256, 512, LDS2, stream>>>(Ackv, Kc, Vc, ws);
  k3<<<64, 512, 0, stream>>>(Awuv, Awo, Bwo, ws);
  k4<<<8, 256, 0, stream>>>(ws, (u16*)d_out);
}
```

```cpp
#include <hip/hip_runtime.h>
#include <stdint.h>

typedef unsigned int u32;
typedef unsigned short u16;
typedef unsigned char u8;
typedef __attribute__((ext_vector_type(4))) unsigned u32x4;
typedef __attribute__((ext_vector_type(2))) unsigned u32x2;
typedef __attribute__((ext_vector_type(4))) float f32x4;
typedef __attribute__((ext_vector_type(2))) float f32x2;
typedef __attribute__((ext_vector_type(16))) float f32x16;
typedef __attribute__((ext_vector_type(8))) __bf16 bf16x8;
typedef __attribute__((ext_vector_type(2))) __bf16 bf16x2;
typedef __attribute__((ext_vector_type(4))) short s16x4;
typedef __attribute__((address_space(3))) s16x4* lds_s16x4p;
#define DI __device__ __forceinline__

#define MIMIC 0
#define STAMP 0
#define BW 4
#define BG 4
#define QSPLIT 1
#define BMAP 1
#define BW0 32
#define BSTAT 28
#define BAHD 3
#define TSLP 4
#define BSYNC 0
#define BPAT 2
#define KVNT 1

constexpr int SK = 8192, HD = 128, DC = 512;
constexpr float SCALE = 0.08838834764831845f;
constexpr float LOG2E = 1.4426950408889634f;

DI f32x4 mfma16(u32x4 a, u32x4 b, f32x4 c) {
  return __builtin_amdgcn_mfma_f32_16x16x32_bf16(__builtin_bit_cast(bf16x8, a), __builtin_bit_cast(bf16x8, b), c, 0, 0, 0);
}
DI f32x16 mfma32(u32x4 a, u32x4 b, f32x16 c) {
  return __builtin_amdgcn_mfma_f32_32x32x16_bf16(__builtin_bit_cast(bf16x8, a), __builtin_bit_cast(bf16x8, b), c, 0, 0, 0);
}
DI u32 cvtpk(float lo, float hi) { return __builtin_bit_cast(u32, __builtin_convertvector(f32x2{lo, hi}, bf16x2)); }
DI float bf16r(float x) { return __uint_as_float(cvtpk(x, x) << 16); }
DI float bflo(u32 h) { return __uint_as_float(h << 16); }
DI float bfhi(u32 h) { return __uint_as_float(h & 0xffff0000u); }
template <int CTRL> DI u32 dppu(u32 x) { return (u32)__builtin_amdgcn_mov_dpp((int)x, CTRL, 0xf, 0xf, true); }
template <int CTRL> DI float dppf(float x) { return __uint_as_float(dppu<CTRL>(__float_as_uint(x))); }
typedef __amdgpu_buffer_rsrc_t rsrc_t;
DI rsrc_t mkrs(const void* p) { return __builtin_amdgcn_make_buffer_rsrc((void*)p, 0, 0x7fffffff, 0x00020000); }
DI u32x4 ldn(rsrc_t r, u32 voff, u32 soff) { return __builtin_amdgcn_raw_buffer_load_b128(r, voff, soff, 2); }
#define WAUX 2
DI u32x4 ldw(rsrc_t r, u32 voff, u32 soff) { return __builtin_amdgcn_raw_buffer_load_b128(r, voff, soff, WAUX); }
DI float x16_sum(float x) { auto s = __builtin_amdgcn_permlane16_swap(__float_as_uint(x), __float_as_uint(x), false, false); return __uint_as_float(s[0]) + __uint_as_float(s[1]); }
DI float x32_sum(float x) { auto t = __builtin_amdgcn_permlane32_swap(__float_as_uint(x), __float_as_uint(x), false, false); return __uint_as_float(t[0]) + __uint_as_float(t[1]); }
DI float x16_max(float x) { auto s = __builtin_amdgcn_permlane16_swap(__float_as_uint(x), __float_as_uint(x), false, false); return fmaxf(__uint_as_float(s[0]), __uint_as_float(s[1])); }
DI float x32_max(float x) { auto t = __builtin_amdgcn_permlane32_swap(__float_as_uint(x), __float_as_uint(x), false, false); return fmaxf(__uint_as_float(t[0]), __uint_as_float(t[1])); }
DI float row_max(float x) { x = fmaxf(x, dppf<0xB1>(x)); x = fmaxf(x, dppf<0x4E>(x)); x = fmaxf(x, dppf<0x141>(x)); return fmaxf(x, dppf<0x128>(x)); }
DI float row_sum(float x) { x += dppf<0xB1>(x); x += dppf<0x4E>(x); x += dppf<0x141>(x); return x + dppf<0x128>(x); }
DI float wave_max(float x) { return x32_max(x16_max(row_max(x))); }
DI float wave_sum(float x) { return x32_sum(x16_sum(row_sum(x))); }
DI float quad_diag_sum(f32x4 d) {
  float s = dppf<0x00>(d[0]);
  s += dppf<0x55>(d[1]);
  s += dppf<0xAA>(d[2]);
  s += dppf<0xFF>(d[3]);
  return s;
}
#define LBAR() do { asm volatile("s_waitcnt lgkmcnt(0)" ::: "memory"); __builtin_amdgcn_s_barrier(); asm volatile("" ::: "memory"); } while (0)
#define SB() __builtin_amdgcn_sched_barrier(0)
DI float ex2(float x) { return __builtin_amdgcn_exp2f(x); }

constexpr int WS_EPOCH = 0;
constexpr int WS_QX = 4096;
constexpr int WS_QABS = WS_QX + 32 * 2048;
constexpr int WS_PAO = WS_QABS + 2 * 32 * 2048;
constexpr int WS_PAML = WS_PAO + 32 * 32 * 2048;
constexpr int WS_PB = WS_PAML + 32 * 32 * 16;
constexpr int WS_PBML = WS_PB + 32 * 16 * 1024;
constexpr int WS_CNT = WS_PBML + 32 * 16 * 16;
constexpr int WS_CTX = WS_CNT + 64 * 128;
constexpr int WS_YA = WS_CTX + 32 * 1024;
constexpr int WS_YB = WS_YA + 512 * 512;
constexpr int WS_END = WS_YB + 512 * 512;
constexpr int WS_STAMP = 8 << 20;
constexpr u32 SPIN_CAP = 1u << 16;

DI u32x4 gld(rsrc_t rs, int off) { asm volatile("" ::: "memory"); return __builtin_amdgcn_raw_buffer_load_b128(rs, off, 0, 16); }
DI void gst(rsrc_t rs, int off, u32 v0, u32 v1, u32 ta, u32 tb) { __builtin_amdgcn_raw_buffer_store_b128(u32x4{v0, ta, v1, tb}, rs, off, 0, 16); }
DI bool gok(u32x4 g, u32 ta, u32 tb) { return g[1] == ta && g[3] == tb; }
#define GWAIT(g_, off_, slp_) { _Pragma("nounroll") for (u32 spin = 0; spin < SPIN_CAP; ++spin) { \
    const bool ok_ = gok(g_, ta, tbg); if (__all(ok_)) break; __builtin_amdgcn_s_sleep(slp_); if (!ok_) g_ = gld(rs, off_); } }
#define USE4(x_) asm volatile("" :: "v"(x_))

#define STAMP_AT(k_) do {} while (0)
#define STAMP_FLUSH(n_) do {} while (0)

struct Sel { u32 e[4], o[4]; };
DI Sel make_sel(int lane) {
  Sel s; const int slot = lane & 7, r0 = slot >> 1, half = slot & 1;
#pragma unroll
  for (int r = 0; r < 4; ++r) {
    s.e[r] = (r == r0) ? (half ? 0x01000c0cu : 0x0c0c0100u) : 0x0c0c0c0cu;
    s.o[r] = (r == r0) ? (half ? 0x03020c0cu : 0x0c0c0302u) : 0x0c0c0c0cu;
  }
  return s;
}
DI u32x4 place(u32 src, const u32 (&sel)[4]) {
  u32x4 a;
#pragma unroll
  for (int r = 0; r < 4; ++r) a[r] = __builtin_amdgcn_perm(0u, src, sel[r]);
  return a;
}
#define FRESH_SEL(name_) int name_##_l = lane; asm volatile("" : "+v"(name_##_l)); const Sel name_ = make_sel(name_##_l);
DI u32 off_a(u32 row, u32 ch) { return 2048u * (row >> 3) + 512u * (ch >> 2) + 64u * (row & 7) + 16u * ((ch & 3) ^ ((row >> 2) & 3)); }
DI u32x2 tr_read(const u8* p) { return __builtin_bit_cast(u32x2, __builtin_amdgcn_ds_read_tr16_b64_v4i16((lds_s16x4p)p)); }

constexpr int L_IMG = 0;
constexpr int L_SX = 65536;
constexpr int L_Q = 131072;

#define SVC_BAR(k_) { asm volatile("s_waitcnt lgkmcnt(0)" ::: "memory"); if (lane == 0) __hip_atomic_fetch_add(&ctl[0], 1u, __ATOMIC_RELAXED, __HIP_MEMORY_SCOPE_WORKGROUP); \
    _Pragma("nounroll") for (u32 spin = 0; spin < (SPIN_CAP << 4); ++spin) { if (__hip_atomic_load(&ctl[0], __ATOMIC_RELAXED, __HIP_MEMORY_SCOPE_WORKGROUP) >= 4u * (k_)) break; __builtin_amdgcn_s_sleep(1); } \
    asm volatile("" ::: "memory"); }
#define SVC_ISSUE() \
  u32x4 xfB[4], xfA[4], wB[4], wA[4], wk[4]; \
  if (wave >= 4) { \
    if (rq == 0 && tid >= 256 && tid < 258) __builtin_amdgcn_raw_buffer_store_b32(0u, rs, WS_CNT + (hq * 2 + (tid & 1)) * 128, 0, 16);     \
    const u32 wo_ = ((128 * sw + (lane >> 1)) * 4096 + hq * 128 + 16 * rq + 8 * (lane & 1)) * 2; \
    const rsrc_t r1 = mkrs(Bwq), r2 = mkrs(Awq), r3 = mkrs(Awuk); \
    _Pragma("unroll") for (int t = 0; t < 4; ++t) { wB[t] = ldn(r1, wo_, t * 32 * 8192); wA[t] = ldn(r2, wo_, t * 32 * 8192); } \
    const int xo = (n & 1) * 512 + 128 * sw + 8 * g; \
    _Pragma("unroll") for (int t = 0; t < 4; ++t) { xfB[t] = *(const u32x4*)(Bx + xo + 32 * t); xfA[t] = *(const u32x4*)(Ax + xo + 32 * t); } \
    const u32 ko_ = ((64 * rq + 16 * sw + ((lane >> 2) & 3)) * 4096 + hq * 128 + 8 * (4 * g + (lane & 3))) * 2; \
    _Pragma("unroll") for (int t = 0; t < 4; ++t) wk[t] = ldn(r3, ko_, t * 4 * 8192); } \
  SB();

#define SVC_RUN() { \
    u8* wi = lds + L_SX + sw * 8192; \
    _Pragma("unroll") for (int t = 0; t < 4; ++t) { *(u32x4*)(wi + t * 1024 + lane * 16) = wB[t]; *(u32x4*)(wi + 4096 + t * 1024 + lane * 16) = wA[t]; } \
    const u8* rp = wi + (8 * g + ((lane & 15) >> 2)) * 32 + 8 * (lane & 3); \
    f32x4 dB = {0.f, 0.f, 0.f, 0.f}, dA = dB; \
    const bool xl = n < 2; \
    _Pragma("unroll") for (int t = 0; t < 4; ++t) { \
      { const u32x2 a = tr_read(rp + t * 1024), b_ = tr_read(rp + t * 1024 + 128); dB = mfma16(xl ? xfB[t] : z4, u32x4{a[0], a[1], b_[0], b_[1]}, dB); } \
      { const u32x2 a = tr_read(rp + 4096 + t * 1024), b_ = tr_read(rp + 4096 + t * 1024 + 128); dA = mfma16(xl ? xfA[t] : z4, u32x4{a[0], a[1], b_[0], b_[1]}, dA); } } \
    float* qp = (float*)(lds + L_Q + 256);   \
    if (lane < 16) { qp[sw * 64 + n] = dB[0]; qp[sw * 64 + 16 + n] = dB[1]; qp[sw * 64 + 32 + n] = dA[0]; qp[sw * 64 + 48 + n] = dA[1]; } \
    SVC_BAR(1) \
    STAMP_AT(5); \
    if (sw == 0 && lane < 16) { f32x4 s = {0.f, 0.f, 0.f, 0.f}; \
      _Pragma("unroll") for (int w_ = 0; w_ < 4; ++w_) s += *(const f32x4*)&qp[w_ * 64 + 4 * lane]; \
      gst(rs, WS_QX + ((hq * 8 + rq) * 16 + lane) * 16, cvtpk(s[0], s[1]), cvtpk(s[2], s[3]), ta, tbg); } \
    if (sw >= 2) {   \
      const int qo_ = WS_QX + (((sw == 2 ? h : hq) * 8 + (lane >> 3)) * 16 + (sw - 2) * 8 + (lane & 7)) * 16; \
      __builtin_amdgcn_s_sleep(8); \
      u32x4 gq_ = gld(rs, qo_); \
      GWAIT(gq_, qo_, 2) \
      *(u32x2*)(lds + L_Q + 2048 + ((sw - 2) * 2 + ((lane >> 2) & 1)) * 256 + (lane >> 3) * 32 + (lane & 3) * 8) = u32x2{gq_[0], gq_[2]}; } \
    SVC_BAR(2)     \
    STAMP_AT(6); \
    const u32x4 qv = *(const u32x4*)(lds + L_Q + 2048 + 512 + ((n >> 2) & 1) * 256 + 16 * (4 * g + (n & 3))); \
    const u32x4 qf0 = (n >> 3) == 0 ? qv : z4, qf1 = (n >> 3) == 1 ? qv : z4; \
    float* qa = (float*)(lds + L_Q + 4096) + sw * 32;     \
    _Pragma("unroll") for (int u = 0; u < 2; ++u) { \
      f32x4 d = {0.f, 0.f, 0.f, 0.f}; \
      d = mfma16(wk[2 * u], qf0, d); d = mfma16(wk[2 * u + 1], qf1, d); \
      const float s = quad_diag_sum(d); \
      if ((lane & 3) == 0) qa[((lane >> 2) & 1) * 16 + 8 * u + 4 * ((lane >> 3) & 1) + g] = s; } \
    asm volatile("s_waitcnt lgkmcnt(0)" ::: "memory"); \
    if (lane < 8) { const f32x4 v = *(const f32x4*)&qa[4 * lane]; \
      gst(rs, WS_QABS + (((lane >> 2) * 32 + hq) * 128 + 16 * rq + 4 * sw + (lane & 3)) * 16, cvtpk(v[0], v[1]), cvtpk(v[2], v[3]), ta, tbg); } } \
  SB();

#define B_SCALE(sc_) sc_ = sc_ * (SCALE * LOG2E);

__global__ void __launch_bounds__(512, 2) fused(const u16* __restrict__ Ax, const u16* __restrict__ Ackv, const u16* __restrict__ Awq, const u16* __restrict__ Awuk,
                                                const u16* __restrict__ Awuv, const u16* __restrict__ Awo, const u16* __restrict__ Bx, const u16* __restrict__ Kc,
                                                const u16* __restrict__ Vc, const u16* __restrict__ Bwq, const u16* __restrict__ Bwo, u16* __restrict__ out, unsigned char* ws) {
  extern __shared__ __attribute__((aligned(16))) unsigned char lds[];
  const int tid = threadIdx.x, lane = tid & 63, wave = __builtin_amdgcn_readfirstlane(tid >> 6);
  const int bx = blockIdx.x, x8 = bx & 7, jj = bx >> 3;
  const int h = jj, role = (x8 + jj) & 7;
  const int hq = 4 * x8 + (jj >> 3), rq = jj & 7;
  const int sw = wave - 4;
  const int g = lane >> 4, n = lane & 15;
  const rsrc_t rs = __builtin_amdgcn_make_buffer_rsrc(ws, 0, WS_END, 0x00020000);
  STAMP_AT(0);
  const u32x2 ep = *(const __attribute__((address_space(4))) u32x2*)(ws + WS_EPOCH);
  const u32 enext = ep[0] + 1u, e1n = ep[1];
  const u32 ta = (enext * 0x9E3779B1u) ^ 0x7F4A7C15u, tbg = ((enext ^ e1n) * 0x85EBCA77u) ^ (e1n >> 7) ^ 0x2545F491u;
  const u32x4 z4 = {0u, 0u, 0u, 0u};
  u32x4 wo0, wo1, wa0, wa1;
#define TAIL_ISSUE() { const u32 o_ = ((h * 128 + (tid >> 3)) * 512 + 64 * role + 8 * (tid & 7)) * 2; const rsrc_t r1 = mkrs(Bwo), r2 = mkrs(Awo); \
    wo0 = ldn(r1, o_, 0); wo1 = ldn(r1, o_, 65536); wa0 = ldn(r2, o_, 0); wa1 = ldn(r2, o_, 65536); SB(); }

  u32* ctl = (u32*)(lds + L_Q);
  u32* tq = (u32*)(lds + L_Q + 8192);
  if (tid < 8) ctl[tid] = 0u;
  if (tid < 128) tq[tid] = 0xffffffffu;
  constexpr int BS = BSTAT, T0 = 14 * BS;
  if (role != 7) {
    if (wave >= 4) {
      SVC_ISSUE()
      LBAR();
      SVC_RUN()
      {
        const int t2 = tid - 256;
        const u32 o_ = ((h * 128 + (t2 >> 3)) * 512 + 64 * role + 8 * (t2 & 7)) * 2; const rsrc_t r1 = mkrs(Bwo), r2 = mkrs(Awo);
        u32x4 a[4], c[4];
#pragma unroll
        for (int t = 0; t < 4; ++t) { a[t] = ldn(r1, o_, t * 32768); c[t] = ldn(r2, o_, t * 32768); }
#pragma unroll
        for (int t = 0; t < 4; ++t) { *(u32x4*)(lds + L_IMG + t * 4096 + t2 * 16) = a[t]; *(u32x4*)(lds + L_IMG + 16384 + t * 4096 + t2 * 16) = c[t]; }
      }
      {
        const u32 coff = lane ? 0x7fffff00u : (u32)(WS_CNT + (h * 2 + (sw >> 1)) * 128);
        int prod = 0;
        _Pragma("nounroll") for (u32 spin = 0; spin < (SPIN_CAP << 4); ++spin) {
          const int c = (int)__builtin_amdgcn_readfirstlane(__hip_atomic_load(&ctl[4 + sw], __ATOMIC_RELAXED, __HIP_MEMORY_SCOPE_WORKGROUP));
          const int want = c + BAHD - BS;
          if (want > prod) {
            if (prod >= 28) { if (lane == 0) __hip_atomic_store(&tq[sw * 32 + prod], 512u, __ATOMIC_RELAXED, __HIP_MEMORY_SCOPE_WORKGROUP); break; }
            const int nc = (want - prod >= 2) ? 2 : 1;
            const int base = T0 + __builtin_amdgcn_readfirstlane(__builtin_amdgcn_raw_ptr_buffer_atomic_add_i32(nc, rs, coff, 0, 0));
            if (lane < nc) __hip_atomic_store(&tq[sw * 32 + prod + lane], (u32)(base + lane), __ATOMIC_RELAXED, __HIP_MEMORY_SCOPE_WORKGROUP);
            prod += nc;
            if (base + nc > 512) break;
          } else __builtin_amdgcn_s_sleep(4);
        }
      }
      LBAR();
    } else {
      constexpr int W0T = BW0 / 8;
      const int b = wave >> 1, w14 = role * 2 + (wave & 1);
      const rsrc_t rk = __builtin_amdgcn_make_buffer_rsrc((void*)((const u8*)Kc + ((size_t)(b * 32 + h) << 21)), 0, 1 << 21, 0x00020000);
      const rsrc_t rv = __builtin_amdgcn_make_buffer_rsrc((void*)((const u8*)Vc + ((size_t)(b * 32 + h) << 21)), 0, 1 << 21, 0x00020000);
      const u32 ko = (((lane >> 2) & 3) * 16 + 4 * g + (lane & 3)) * 16, vo = lane * 16;
      u32x4 U[W0T][8];
#define B_LK(X_, t_) { const u32 so_ = (u32)(t_) * 4096u; _Pragma("unroll") for (int q_ = 0; q_ < 4; ++q_) X_[q_] = ldn(rk, ko + q_ * 1024, so_); SB(); }
#define B_LV(X_, t_) { const u32 so_ = (u32)(t_) * 4096u; _Pragma("unroll") for (int q_ = 0; q_ < 4; ++q_) X_[4 + q_] = ldn(rv, vo + q_ * 1024, so_); SB(); }
      LBAR();
      B_LK(U[0], w14) B_LV(U[0], w14)
#pragma unroll
      for (int p = 1; p < W0T; ++p) { USE4(U[p - 1][3]); SB(); B_LK(U[p], w14 + 14 * p) USE4(U[p - 1][7]); SB(); B_LV(U[p], w14 + 14 * p) }
      STAMP_AT(4);
      _Pragma("nounroll") for (u32 spin = 0; spin < (SPIN_CAP << 4); ++spin) { if (__hip_atomic_load(&ctl[0], __ATOMIC_RELAXED, __HIP_MEMORY_SCOPE_WORKGROUP) >= 8u) break; __builtin_amdgcn_s_sleep(2); }
      asm volatile("" ::: "memory");
      STAMP_AT(1);
      u32x4 qf[4];
      { const u32x4 qv = *(const u32x4*)(lds + L_Q + 2048 + b * 256 + 16 * (4 * g + (n & 3)));
#pragma unroll
        for (int t = 0; t < 4; ++t) qf[t] = (n >> 2) == t ? qv : z4; }
      u32 msk[4];
#pragma unroll
      for (int r = 0; r < 4; ++r) msk[r] = (n < 8 && (n >> 1) == r) ? ((n & 1) ? 0xffff0000u : 0x0000ffffu) : 0u;
      float m = -INFINITY, l = 0.f; f32x4 acc = {0.f, 0.f, 0.f, 0.f};
      SB();
#define B_QK(X_) f32x4 d = {0.f, 0.f, 0.f, 0.f}; _Pragma("unroll") for (int t = 0; t < 4; ++t) d = mfma16(X_[t], qf[t], d); asm volatile("" : "+v"(d) :: "memory"); SB();
#define B_PV1(X_, t_) { u32x4 a; const u32 pb = (u32)__builtin_amdgcn_mov_dpp((int)p2, 0x150 + 4 * (t_), 0xf, 0xf, true); \
        a[0] = pb & msk[0]; a[1] = pb & msk[1]; a[2] = pb & msk[2]; a[3] = pb & msk[3]; acc = mfma16(a, X_[4 + (t_)], acc); }
#define B_SMPV(X_) { float sc = quad_diag_sum(d); B_SCALE(sc) \
        if (__any(sc > m)) { const float mn = __builtin_ceilf(wave_max(sc)); const float al = ex2(m - mn); l *= al; acc *= al; m = mn; } \
        const float p = ex2(sc - m); l += p; const u32 p2 = cvtpk(p, p); \
        B_PV1(X_, 0) B_PV1(X_, 1) B_PV1(X_, 2) B_PV1(X_, 3) asm volatile("" : "+v"(acc) :: "memory"); SB(); }
#define B_NEXT(tn_, kk_) u32 tn_ = (u32)(w14 + 14 * (kk_)); \
        if ((kk_) >= BS) { \
          _Pragma("nounroll") for (u32 spin = 0; spin < (SPIN_CAP << 4); ++spin) { \
            tn_ = __builtin_amdgcn_readfirstlane(__hip_atomic_load(&tq[wave * 32 + (kk_) - BS], __ATOMIC_RELAXED, __HIP_MEMORY_SCOPE_WORKGROUP)); \
            if (tn_ != 0xffffffffu) break; \
            __builtin_amdgcn_s_sleep(1); } \
          tn_ = tn_ < 512u ? tn_ : 512u; } \
        if (lane == 0) __hip_atomic_store(&ctl[4 + wave], (u32)(kk_), __ATOMIC_RELAXED, __HIP_MEMORY_SCOPE_WORKGROUP);
#define B_TS(k_)
      { B_QK(U[0]) B_LK(U[0], w14 + 14 * W0T) B_SMPV(U[0]) B_LV(U[0], w14 + 14 * W0T) }
      { B_QK(U[1]) B_LK(U[1], w14 + 14 * (W0T + 1)) B_SMPV(U[1]) B_LV(U[1], w14 + 14 * (W0T + 1)) }
#pragma unroll
      for (int pp = 2; pp < W0T; ++pp) { B_QK(U[pp]) B_SMPV(U[pp]) }
      bool vb = true;
      _Pragma("nounroll") for (int k = W0T; k < BS + 30; k += 2) {
        u32 t0 = 512u, t1 = 512u;
        if (vb) { B_NEXT(t0_, k + 2) t0 = t0_; }
        { B_QK(U[0]) B_LK(U[0], t0) B_SMPV(U[0]) B_LV(U[0], t0) }
        B_TS(k)
        if (t0 < 512u) { B_NEXT(t1_, k + 3) t1 = t1_; }
        { B_QK(U[1]) if (!vb) d[0] = -INFINITY; B_LK(U[1], t1) B_SMPV(U[1]) B_LV(U[1], t1) }
        B_TS(k + 1)
        vb = t1 < 512u;
        if (t0 >= 512u) break;
      }
      STAMP_AT(2);
      l = wave_sum(l) * 0.25f;
      float* pml = (float*)(lds + 32768);
      float* pacc = (float*)(lds + 32768 + 64);
      if (lane < 32) *(f32x4*)&pacc[wave * 128 + 8 * n + 4 * g] = acc;
      if (lane == 0) *(f32x2*)&pml[wave * 2] = f32x2{m, l};
      LBAR();
      if (wave < 2) {
        const f32x2 v0 = *(const f32x2*)&pml[wave * 4], v1 = *(const f32x2*)&pml[wave * 4 + 2];
        const float M = fmaxf(v0[0], v1[0]), a0 = ex2(v0[0] - M), a1 = ex2(v1[0] - M);
        const f32x2 o2 = a0 * *(const f32x2*)&pacc[(2 * wave) * 128 + 2 * lane] + a1 * *(const f32x2*)&pacc[(2 * wave + 1) * 128 + 2 * lane];
        gst(rs, WS_PB + (((h * 2 + wave) * 8 + role) * 64 + lane) * 16, __float_as_uint(o2[0]), __float_as_uint(o2[1]), ta, tbg);
        if (lane == 0) gst(rs, WS_PBML + ((h * 2 + wave) * 8 + role) * 16, __float_as_uint(M), __float_as_uint(a0 * v0[1] + a1 * v1[1]), ta, tbg);
      }
      STAMP_AT(3);
    }
  } else {
    const int bA = h >> 4, sp = h & 15;
    const int kg = wave >> 2, cs = wave & 3;
    const int hh = lane & 31, hf = lane >> 5;
    u32x4 st[2][8];
    const rsrc_t rc = mkrs(Ackv + ((size_t)bA * SK + 512 * sp + 32 * kg) * DC + 128 * cs);
    const u32 co = g * 1024 + n * 16;
#define A_ISSUE(it_) { _Pragma("unroll") for (int i_ = 0; i_ < 8; ++i_) st[(it_) & 1][i_] = ldn(rc, co, ((it_) * 64 + 4 * i_) * 1024); SB(); }
    u8* img = lds + L_IMG + wave * 8192;
#define A_STAGE(it_) { _Pragma("unroll") for (int i = 0; i < 8; ++i) *(u32x4*)(img + off_a(4 * i + g, n)) = st[(it_) & 1][i]; asm volatile("" ::: "memory"); SB(); }
    {
      SVC_ISSUE()
      LBAR();
      if (wave >= 4) { SVC_RUN() A_ISSUE(0) } else { A_ISSUE(0) USE4(st[0][7]); SB(); }
      A_ISSUE(1)
    }
    STAMP_AT(1);
    A_STAGE(0)
    u32x4 qh[8];
    {
      u8* qhi = lds + L_SX;
      LBAR();
      u32x4 gq[8];
#pragma unroll
      for (int k = 0; k < 8; ++k) gq[k] = gld(rs, WS_QABS + ((bA * 32 + 4 * wave) * 128 + k * 64 + lane) * 16);
      SB();
      A_ISSUE(2)
#pragma unroll
      for (int k = 0; k < 8; ++k) GWAIT(gq[k], WS_QABS + ((bA * 32 + 4 * wave) * 128 + k * 64 + lane) * 16, 16)
#pragma unroll
      for (int k = 0; k < 8; ++k) {
        const int idx = (lane >> 1) & 15, hd = 4 * wave + (k >> 1);
        *(u32x2*)(qhi + ((2 * (k & 1) + (lane >> 5)) * 16 + idx) * 512 + ((hd ^ (idx & 7)) * 16) + (lane & 1) * 8) = u32x2{gq[k][0], gq[k][2]};
      }
      LBAR();
#pragma unroll
      for (int s = 0; s < 8; ++s) qh[s] = *(const u32x4*)(qhi + (cs * 16 + 2 * s + hf) * 512 + ((hh ^ ((2 * s + hf) & 7)) * 16));
      LBAR();
    }
    STAMP_AT(2);
    f32x16 o[4];
#pragma unroll
    for (int c = 0; c < 4; ++c)
#pragma unroll
      for (int r = 0; r < 16; ++r) o[c][r] = 0.f;
    float m = -INFINITY, l = 0.f;
    float* sx = (float*)(lds + L_SX);
    u32x4 wv[16];
#pragma unroll
    for (int it = 0; it < 8; ++it) {
      f32x16 sa;
#pragma unroll
      for (int r = 0; r < 16; ++r) sa[r] = 0.f;
#pragma unroll
      for (int ks = 0; ks < 8; ++ks) { const u32x4 a = *(const u32x4*)(img + off_a(hh, 2 * ks + hf)); sa = mfma32(a, qh[ks], sa); }
      float* sb = sx + (it & 1) * 8192;
#pragma unroll
      for (int q = 0; q < 4; ++q) *(f32x4*)&sb[wave * 1024 + q * 256 + lane * 4] = f32x4{sa[4 * q], sa[4 * q + 1], sa[4 * q + 2], sa[4 * q + 3]};
      LBAR();
#pragma unroll
      for (int w = 0; w < 4; ++w)
#pragma unroll
        for (int q = 0; q < 4; ++q) { const f32x4 v = *(const f32x4*)&sb[(kg * 4 + w) * 1024 + q * 256 + lane * 4];
          if (w == 0) { sa[4 * q] = v[0]; sa[4 * q + 1] = v[1]; sa[4 * q + 2] = v[2]; sa[4 * q + 3] = v[3]; }
          else { sa[4 * q] += v[0]; sa[4 * q + 1] += v[1]; sa[4 * q + 2] += v[2]; sa[4 * q + 3] += v[3]; }
          if (w == 1 && q == 3) SB(); }
      constexpr float CS = SCALE * LOG2E;
      float mx = sa[0];
#pragma unroll
      for (int r = 1; r < 16; ++r) mx = fmaxf(mx, sa[r]);
      mx = x32_max(mx) * CS;
      if (__any(mx > m)) {
        const float mn = fmaxf(m, mx), al = ex2(m - mn);
        m = mn; l *= al;
#pragma unroll
        for (int c = 0; c < 4; ++c) o[c] *= al;
      }
      u32x4 pf[2];
#pragma unroll
      for (int r = 0; r < 16; r += 2) {
        const float p0 = ex2(fmaf(sa[r], CS, -m)), p1 = ex2(fmaf(sa[r + 1], CS, -m));
        l += p0 + p1; pf[r >> 3][(r >> 1) & 3] = cvtpk(p0, p1);
      }
      const u32 blk = (lane >> 4) & 1, q4 = (lane & 15) >> 2, p4 = lane & 3;
#pragma unroll
      for (int ct = 0; ct < 4; ++ct)
#pragma unroll
        for (int ks = 0; ks < 2; ++ks) {
          const u32x2 lo = tr_read(img + off_a(16 * ks + 4 * hf + q4, 4 * ct + 2 * blk + (p4 >> 1)) + 8 * (p4 & 1));
          const u32x2 hi = tr_read(img + off_a(16 * ks + 8 + 4 * hf + q4, 4 * ct + 2 * blk + (p4 >> 1)) + 8 * (p4 & 1));
          o[ct] = mfma32(u32x4{lo[0], lo[1], hi[0], hi[1]}, pf[ks], o[ct]);
        }
      if (it + 1 < 8) {
        A_STAGE(it + 1)
        if (it + 3 < 8) A_ISSUE(it + 3)
      }
    }
    STAMP_AT(3);
    {
      l = x32_sum(l);
      LBAR();
      float* ml = (float*)(lds + L_Q + 256);
      float* ob = (float*)(lds + L_IMG);
      u8* om = lds + L_SX;
      if (cs == 0 && lane < 32) *(f32x2*)&ml[(kg * 32 + hh) * 2] = f32x2{m, l};
      if (kg == 1) {
#pragma unroll
        for (int ct = 0; ct < 4; ++ct)
#pragma unroll
          for (int q = 0; q < 4; ++q) *(f32x4*)&ob[(((cs * 4 + ct) * 4 + q) * 64 + lane) * 4] = f32x4{o[ct][4 * q], o[ct][4 * q + 1], o[ct][4 * q + 2], o[ct][4 * q + 3]};
      }
      LBAR();
      if (kg == 0) {
        const f32x2 v1 = *(const f32x2*)&ml[(32 + hh) * 2];
        const float mn = fmaxf(m, v1[0]), a0 = ex2(m - mn), a1 = ex2(v1[0] - mn);
#pragma unroll
        for (int ct = 0; ct < 4; ++ct)
#pragma unroll
          for (int q = 0; q < 4; ++q) {
            const f32x4 o1 = *(const f32x4*)&ob[(((cs * 4 + ct) * 4 + q) * 64 + lane) * 4];
            f32x4 v;
#pragma unroll
            for (int e = 0; e < 4; ++e) v[e] = o[ct][4 * q + e] * a0 + o1[e] * a1;
            const int c4 = 32 * cs + 8 * ct + 2 * q + hf;
            *(u32x2*)(om + hh * 1024 + ((c4 ^ (hh & 15)) * 8)) = u32x2{cvtpk(v[0], v[1]), cvtpk(v[2], v[3])};
          }
        m = mn; l = l * a0 + v1[1] * a1;
      }
      LBAR();
#pragma unroll
      for (int i = 0; i < 8; ++i) {
        const int gi = i * 512 + tid, h2 = gi >> 7, c4 = gi & 127;
        const u32x2 v = *(const u32x2*)(om + h2 * 1024 + ((c4 ^ (h2 & 15)) * 8));
        gst(rs, WS_PAO + (((h2 * 2 + bA) * 16 + sp) * 128 + c4) * 16, v[0], v[1], ta, tbg);
      }
      asm volatile("s_waitcnt vmcnt(0)" ::: "memory");
      LBAR();
      if (wave == 0 && lane < 32) gst(rs, WS_PAML + ((hh * 2 + bA) * 16 + sp) * 16, __float_as_uint(m), __float_as_uint(l), ta, tbg);
    }
    SB();
#pragma unroll
    for (int t = 0; t < 16; ++t) wv[t] = ldn(mkrs(Awuv), ((64 * wave + 16 * g) * 4096 + h * 128 + 8 * n) * 2, t * 8192);
    SB();
    TAIL_ISSUE()
    STAMP_AT(4);
    {
      const int b = tid >> 8, sh = (tid >> 7) & 1, c4 = tid & 127;
      const int mo = WS_PAML + ((h * 2 + b) * 16 + n) * 16;
      u32x4 gm = gld(rs, mo);
      GWAIT(gm, mo, 16)
      u32x4 go[8];
#pragma unroll
      for (int s = 0; s < 8; ++s) go[s] = gld(rs, WS_PAO + (((h * 2 + b) * 16 + 8 * sh + s) * 128 + c4) * 16);
#pragma unroll
      for (int s = 0; s < 8; ++s) GWAIT(go[s], WS_PAO + (((h * 2 + b) * 16 + 8 * sh + s) * 128 + c4) * 16, 4)
      const float ms = __uint_as_float(gm[0]), ls = __uint_as_float(gm[2]);
      const float M = row_max(ms);
      float wt = ex2(ms - M);
      const float L = row_sum(wt * ls);
      wt = wt / L;
      if (sh) wt = dppf<0x128>(wt);
      f32x4 cv = {0.f, 0.f, 0.f, 0.f};
#define A3_ACC(s_) { const float w_ = dppf<0x150 + (s_)>(wt); cv += w_ * f32x4{bflo(go[s_][0]), bfhi(go[s_][0]), bflo(go[s_][2]), bfhi(go[s_][2])}; }
      A3_ACC(0) A3_ACC(1) A3_ACC(2) A3_ACC(3) A3_ACC(4) A3_ACC(5) A3_ACC(6) A3_ACC(7)
      float* cc = (float*)(lds + L_Q + 1024);
      *(f32x4*)&cc[(sh * 2 + b) * 512 + 4 * c4] = cv;
      LBAR();
      FRESH_SEL(sel)
      u32 xh[8], xl[8];
#pragma unroll
      for (int k = 0; k < 4; ++k) {
        const f32x4 v = *(const f32x4*)&cc[(n >> 3) * 512 + 64 * wave + 16 * g + 4 * k] + *(const f32x4*)&cc[1024 + (n >> 3) * 512 + 64 * wave + 16 * g + 4 * k];
        xh[2 * k] = cvtpk(v[0], v[1]); xh[2 * k + 1] = cvtpk(v[2], v[3]);
        xl[2 * k] = cvtpk(v[0] - bflo(xh[2 * k]), v[1] - bfhi(xh[2 * k])); xl[2 * k + 1] = cvtpk(v[2] - bflo(xh[2 * k + 1]), v[3] - bfhi(xh[2 * k + 1]));
      }
      f32x4 ca = {0.f, 0.f, 0.f, 0.f};
#pragma unroll
      for (int t = 0; t < 16; ++t) {
        ca = mfma16(place(xh[t >> 1], (t & 1) ? sel.o : sel.e), wv[t], ca);
        ca = mfma16(place(xl[t >> 1], (t & 1) ? sel.o : sel.e), wv[t], ca);
      }
      float* red = (float*)(lds + L_SX + 32768);
      *(f32x4*)&red[wave * 256 + (g >> 1) * 128 + 8 * n + 4 * (g & 1)] = ca;
      LBAR();
      if (wave == 0) {
        f32x4 s = {0.f, 0.f, 0.f, 0.f};
#pragma unroll
        for (int w = 0; w < 8; ++w) s += *(const f32x4*)&red[w * 256 + 4 * lane];
        gst(rs, WS_CTX + (h * 64 + lane) * 16, cvtpk(s[0], s[1]), cvtpk(s[2], s[3]), ta, tbg);
      }
    }
    *(u32x4*)(lds + L_IMG + tid * 16) = wo0; *(u32x4*)(lds + L_IMG + 8192 + tid * 16) = wo1;
    *(u32x4*)(lds + L_IMG + 16384 + tid * 16) = wa0; *(u32x4*)(lds + L_IMG + 24576 + tid * 16) = wa1;
    STAMP_AT(9);
  }

  {
    u8* wi = lds + L_IMG;
    u32* ob = (u32*)(lds + L_Q + 16384);
    u32x4 gm, ga[7];
    const int b = wave & 1;
    const int l7 = (lane & 7) < 7 ? (lane & 7) : 0;
    const int mo = WS_PBML + ((h * 2 + b) * 8 + l7) * 16;
    if (wave >= 4 && wave < 6) {
      gm = gld(rs, mo);
#pragma unroll
      for (int s = 0; s < 7; ++s) ga[s] = gld(rs, WS_PB + (((h * 2 + b) * 8 + s) * 64 + lane) * 16);
    } else if (wave == 6) gm = gld(rs, WS_CTX + (h * 64 + lane) * 16);
    if (wave >= 4 && wave < 6) {
      GWAIT(gm, mo, TSLP)
#pragma unroll
      for (int s = 0; s < 7; ++s) if (!gok(ga[s], ta, tbg)) ga[s] = gld(rs, WS_PB + (((h * 2 + b) * 8 + s) * 64 + lane) * 16);
#pragma unroll
      for (int s = 0; s < 7; ++s) GWAIT(ga[s], WS_PB + (((h * 2 + b) * 8 + s) * 64 + lane) * 16, 1)
      const float ms = __uint_as_float(gm[0]), ls = (lane & 7) < 7 ? __uint_as_float(gm[2]) : 0.f;
      float M = fmaxf(ms, dppf<0xB1>(ms)); M = fmaxf(M, dppf<0x4E>(M)); M = fmaxf(M, dppf<0x141>(M));
      float wt = ex2(ms - M);
      float L = wt * ls; L += dppf<0xB1>(L); L += dppf<0x4E>(L); L += dppf<0x141>(L);
      wt = wt / L;
      float o0 = 0.f, o1 = 0.f;
#define H1_ACC(s_) { const float w_ = dppf<0x150 + (s_)>(wt); o0 += w_ * __uint_as_float(ga[s_][0]); o1 += w_ * __uint_as_float(ga[s_][2]); }
      H1_ACC(0) H1_ACC(1) H1_ACC(2) H1_ACC(3) H1_ACC(4) H1_ACC(5) H1_ACC(6)
      ob[b * 64 + lane] = cvtpk(o0, o1);
    } else if (wave == 6) {
      GWAIT(gm, WS_CTX + (h * 64 + lane) * 16, 4)
      *(u32x2*)&ob[128 + 2 * lane] = u32x2{gm[0], gm[2]};
    }
    LBAR();
    STAMP_AT(7);
    {
      const int mt = wave >> 2, ct = wave & 3;
      const u8* rp = wi + mt * 16384 + (8 * g + ((lane & 15) >> 2)) * 128 + 32 * ct + 8 * (lane & 3);
      f32x4 y = {0.f, 0.f, 0.f, 0.f};
#pragma unroll
      for (int ks = 0; ks < 4; ++ks) {
        const u32x2 a = tr_read(rp + ks * 4096), bq = tr_read(rp + ks * 4096 + 512);
        const u32x4 of = *(const u32x4*)&ob[mt * 128 + (n & 1) * 64 + 16 * ks + 4 * g];
        y = mfma16(n < 2 ? of : z4, u32x4{a[0], a[1], bq[0], bq[1]}, y);
      }
      if (lane < 16) gst(rs, (mt ? WS_YA : WS_YB) + ((64 * role + 16 * ct + n) * 32 + h) * 16, __float_as_uint(y[0]), __float_as_uint(y[1]), ta, tbg);
    }
    if ((wave & 3) == 0) {
      const int yo = (wave ? WS_YA : WS_YB) + (2 * bx * 32 + lane) * 16;
      u32x4 gy = gld(rs, yo);
      GWAIT(gy, yo, TSLP)
      const float s0 = x16_sum(row_sum(__uint_as_float(gy[0]))), s1 = x16_sum(row_sum(__uint_as_float(gy[2])));
      const float t0 = __shfl(s0, 32), t1 = __shfl(s1, 32);
      if (lane == 0) {
        u16* op = out + (wave ? 0 : 512) + 2 * bx;
        *(u32*)op = cvtpk(s0, t0); *(u32*)(op + 1024) = cvtpk(s1, t1);
      }
    }
    if (bx == 0) {
      LBAR();
      if (tid == 0) __builtin_amdgcn_raw_buffer_store_b128(u32x4{enext, e1n, 0u, 0u}, rs, WS_EPOCH, 0, 16);
    }
    STAMP_AT(8);
    STAMP_FLUSH(10);
  }
}

extern "C" void kernel_launch(void* const* d_in, const int* in_sizes, int n_in, void* d_out, int out_size, void* d_ws, size_t ws_size, hipStream_t stream) {
  constexpr int LDSB = 163840;
  (void)hipFuncSetAttribute((const void*)fused, hipFuncAttributeMaxDynamicSharedMemorySize, LDSB);
  fused<<<256, 512, LDSB, stream>>>((const u16*)d_in[0], (const u16*)d_in[1], (const u16*)d_in[2], (const u16*)d_in[3], (const u16*)d_in[4], (const u16*)d_in[5],
                                    (const u16*)d_in[6], (const u16*)d_in[7], (const u16*)d_in[8], (const u16*)d_in[9], (const u16*)d_in[10], (u16*)d_out, (unsigned char*)d_ws);
}
```

```cpp
#include <hip/hip_runtime.h>
#include <stdint.h>

typedef unsigned int u32;
typedef unsigned short u16;
typedef unsigned char u8;
typedef __attribute__((ext_vector_type(4))) unsigned u32x4;
typedef __attribute__((ext_vector_type(2))) unsigned u32x2;
typedef __attribute__((ext_vector_type(4))) float f32x4;
typedef __attribute__((ext_vector_type(2))) float f32x2;
typedef __attribute__((ext_vector_type(2))) double f64x2;
typedef __attribute__((ext_vector_type(16))) float f32x16;
typedef __attribute__((ext_vector_type(8))) __bf16 bf16x8;
typedef __attribute__((ext_vector_type(2))) __bf16 bf16x2;
typedef __attribute__((ext_vector_type(4))) short s16x4;
typedef __attribute__((address_space(3))) s16x4* lds_s16x4p;
typedef __amdgpu_buffer_rsrc_t rsrc_t;
#define DI __device__ __forceinline__

#define STAMP 0
#define BSTAT 28
#define BAHD 3
#define BW0 32
#define BPF 2
#define NOA 0
#define TSLP 4

constexpr int TQN = 64;
constexpr int SK = 8192, DC = 512;
constexpr float CS = 0.08838834764831845f * 1.4426950408889634f;

DI f32x4 mfma16(u32x4 a, u32x4 b, f32x4 c) {
  return __builtin_amdgcn_mfma_f32_16x16x32_bf16(__builtin_bit_cast(bf16x8, a), __builtin_bit_cast(bf16x8, b), c, 0, 0, 0);
}
DI f32x16 mfma32(u32x4 a, u32x4 b, f32x16 c) {
  return __builtin_amdgcn_mfma_f32_32x32x16_bf16(__builtin_bit_cast(bf16x8, a), __builtin_bit_cast(bf16x8, b), c, 0, 0, 0);
}
DI u32 cvtpk(float lo, float hi) { return __builtin_bit_cast(u32, __builtin_convertvector(f32x2{lo, hi}, bf16x2)); }
DI float bflo(u32 h) { return __uint_as_float(h << 16); }
DI float bfhi(u32 h) { return __uint_as_float(h & 0xffff0000u); }
template <int CTRL> DI u32 dppu(u32 x) { return (u32)__builtin_amdgcn_mov_dpp((int)x, CTRL, 0xf, 0xf, true); }
template <int CTRL> DI float dppf(float x) { return __uint_as_float(dppu<CTRL>(__float_as_uint(x))); }
DI rsrc_t mkrs(const void* p, u32 bytes) { return __builtin_amdgcn_make_buffer_rsrc((void*)p, 0, bytes, 0x00020000); }
DI u32x4 ldn(rsrc_t r, u32 voff, u32 soff) { return __builtin_amdgcn_raw_buffer_load_b128(r, voff, soff, 2); }
DI float x16_sum(float x) { auto s = __builtin_amdgcn_permlane16_swap(__float_as_uint(x), __float_as_uint(x), false, false); return __uint_as_float(s[0]) + __uint_as_float(s[1]); }
DI float x32_sum(float x) { auto t = __builtin_amdgcn_permlane32_swap(__float_as_uint(x), __float_as_uint(x), false, false); return __uint_as_float(t[0]) + __uint_as_float(t[1]); }
DI float x16_max(float x) { auto s = __builtin_amdgcn_permlane16_swap(__float_as_uint(x), __float_as_uint(x), false, false); return fmaxf(__uint_as_float(s[0]), __uint_as_float(s[1])); }
DI float x32_max(float x) { auto t = __builtin_amdgcn_permlane32_swap(__float_as_uint(x), __float_as_uint(x), false, false); return fmaxf(__uint_as_float(t[0]), __uint_as_float(t[1])); }
DI float row_max(float x) { x = fmaxf(x, dppf<0xB1>(x)); x = fmaxf(x, dppf<0x4E>(x)); x = fmaxf(x, dppf<0x141>(x)); return fmaxf(x, dppf<0x128>(x)); }
DI float row_sum(float x) { x += dppf<0xB1>(x); x += dppf<0x4E>(x); x += dppf<0x141>(x); return x + dppf<0x128>(x); }
DI float wave_max(float x) { return x32_max(x16_max(row_max(x))); }
DI float quad_diag_sum(f32x4 d) { float s = dppf<0x00>(d[0]); s += dppf<0x55>(d[1]); s += dppf<0xAA>(d[2]); s += dppf<0xFF>(d[3]); return s; }
DI float ex2(float x) { return __builtin_amdgcn_exp2f(x); }
DI int idiff(float a, float b) { return (int)fmaxf(a - b, -4096.f); }
#define LBAR() do { asm volatile("s_waitcnt lgkmcnt(0)" ::: "memory"); __builtin_amdgcn_s_barrier(); asm volatile("" ::: "memory"); } while (0)
#define SB() __builtin_amdgcn_sched_barrier(0)
#define USE4(x_) asm volatile("" :: "v"(x_))
#define LDS_LD(p_) __hip_atomic_load(p_, __ATOMIC_RELAXED, __HIP_MEMORY_SCOPE_WORKGROUP)
#define LDS_ST(p_, v_) __hip_atomic_store(p_, v_, __ATOMIC_RELAXED, __HIP_MEMORY_SCOPE_WORKGROUP)

constexpr int WS_EPOCH = 0;
constexpr int WS_QX = 4096;
constexpr int WS_QABS = WS_QX + 32 * 2048;
constexpr int WS_PAO = WS_QABS + 2 * 32 * 2048;
constexpr int WS_PAML = WS_PAO + 32 * 64 * 1024;
constexpr int WS_PB = WS_PAML + 32 * 64 * 16;
constexpr int WS_PBML = WS_PB + 32 * 8 * 1024;
constexpr int WS_CNT = WS_PBML + 32 * 8 * 32;
constexpr int WS_CTX = WS_CNT + 64 * 128;
constexpr int WS_YA = WS_CTX + 32 * 4096;
constexpr int WS_YB = WS_YA + 512 * 512;
constexpr int WS_END = WS_YB + 512 * 512;
constexpr int WS_STAMP = 8 << 20;
constexpr u32 SPIN_CAP = 1u << 16;

DI u32x4 gld(rsrc_t rs, int off) { asm volatile("" ::: "memory"); return __builtin_amdgcn_raw_buffer_load_b128(rs, off, 0, 16); }
DI void gst(rsrc_t rs, int off, u32 v0, u32 v1, u32 ta, u32 tb) { __builtin_amdgcn_raw_buffer_store_b128(u32x4{v0, ta, v1, tb}, rs, off, 0, 16); }
DI bool gok(u32x4 g, u32 ta, u32 tb) { return g[1] == ta && g[3] == tb; }
#define GWAIT(g_, off_, slp_) { _Pragma("nounroll") for (u32 spin = 0; spin < SPIN_CAP; ++spin) { \
    const bool ok_ = gok(g_, ta, tbg); if (__all(ok_)) break; __builtin_amdgcn_s_sleep(slp_); if (!ok_) g_ = gld(rs, off_); } }

#define STAMP_AT(k_) do {} while (0)
#define STAMP_FLUSH(n_) do {} while (0)

struct Sel { u32 e[4], o[4]; };
DI Sel make_sel(int lane) {
  Sel s; const int slot = lane & 7, r0 = slot >> 1, half = slot & 1;
#pragma unroll
  for (int r = 0; r < 4; ++r) {
    s.e[r] = (r == r0) ? (half ? 0x01000c0cu : 0x0c0c0100u) : 0x0c0c0c0cu;
    s.o[r] = (r == r0) ? (half ? 0x03020c0cu : 0x0c0c0302u) : 0x0c0c0c0cu;
  }
  return s;
}
DI u32x4 place(u32 src, const u32 (&sel)[4]) {
  u32x4 a;
#pragma unroll
  for (int r = 0; r < 4; ++r) a[r] = __builtin_amdgcn_perm(0u, src, sel[r]);
  return a;
}
#define FRESH_SEL(name_) int name_##_l = lane; asm volatile("" : "+v"(name_##_l)); const Sel name_ = make_sel(name_##_l);
DI u32 off_a(u32 row, u32 ch) { return 2048u * (row >> 3) + 512u * (ch >> 2) + 64u * (row & 7) + 16u * ((ch & 3) ^ ((row >> 2) & 3)); }
DI u32x2 tr_read(const u8* p) { return __builtin_bit_cast(u32x2, __builtin_amdgcn_ds_read_tr16_b64_v4i16((lds_s16x4p)p)); }

constexpr int L_IMG = 0;
constexpr int L_SX = 65536;
constexpr int L_Q = 131072;

#define SVC_BAR(k_) { asm volatile("s_waitcnt lgkmcnt(0)" ::: "memory"); if (lane == 0) __hip_atomic_fetch_add(&ctl[0], 1u, __ATOMIC_RELAXED, __HIP_MEMORY_SCOPE_WORKGROUP); \
    _Pragma("nounroll") for (u32 spin = 0; spin < (SPIN_CAP << 4); ++spin) { if (LDS_LD(&ctl[0]) >= 4u * (k_)) break; __builtin_amdgcn_s_sleep(1); } \
    asm volatile("" ::: "memory"); }

__global__ void __launch_bounds__(512, 2) fused(const u16* __restrict__ Ax, const u16* __restrict__ Ackv, const u16* __restrict__ Awq, const u16* __restrict__ Awuk,
                                                const u16* __restrict__ Awuv, const u16* __restrict__ Awo, const u16* __restrict__ Bx, const u16* __restrict__ Kc,
                                                const u16* __restrict__ Vc, const u16* __restrict__ Bwq, const u16* __restrict__ Bwo, u16* __restrict__ out, unsigned char* ws) {
  extern __shared__ __attribute__((aligned(16))) unsigned char lds[];
  const int tid = threadIdx.x, lane = tid & 63, wave = __builtin_amdgcn_readfirstlane(tid >> 6);
  const int bx = blockIdx.x, x8 = bx & 7, jj = bx >> 3;
  const int h = jj, role = (x8 + jj) & 7;
  const int hq = 4 * x8 + (jj >> 3), rq = jj & 7;
  const int sw = wave - 4;
  const int g = lane >> 4, n = lane & 15;
  const bool isA = role >= 6;
  const rsrc_t rs = mkrs(ws, WS_END);
  STAMP_AT(0);
  const u32x2 ep = *(const __attribute__((address_space(4))) u32x2*)(ws + WS_EPOCH);
  const u32 enext = ep[0] + 1u, e1n = ep[1];
  const u32 ta = (enext * 0x9E3779B1u) ^ 0x7F4A7C15u, tbg = ((enext ^ e1n) * 0x85EBCA77u) ^ (e1n >> 7) ^ 0x2545F491u;
  const u32x4 z4 = {0u, 0u, 0u, 0u};
  u32* ctl = (u32*)(lds + L_Q);
  u32* tq = (u32*)(lds + L_Q + 8192);
  if (tid < 8) ctl[tid] = 0u;
  if (tid < TQN) tq[tid] = 0xffffffffu;
  constexpr int T0 = 3 * BSTAT;
  constexpr int W0T = BW0 / 8;
  const int bs = isA ? 2 : BSTAT;

  const int bS = (0x66 >> role) & 1, slot = role >> 1;
  const rsrc_t rk = mkrs((const u8*)Kc + ((size_t)(bS * 32 + h) << 21), 1 << 21), rv = mkrs((const u8*)Vc + ((size_t)(bS * 32 + h) << 21), 1 << 21);
  const u32 ko = (((lane >> 2) & 3) * 16 + 4 * g + (lane & 3)) * 16 + (wave & 3) * 4096, vo = lane * 16 + (wave & 3) * 4096;
#define B_LK(X_, t_) { const u32 so_ = (u32)(t_) * 16384u; _Pragma("unroll") for (int q_ = 0; q_ < 4; ++q_) X_[q_] = ldn(rk, ko + q_ * 1024, so_); SB(); }
#define B_LV(X_, t_) { const u32 so_ = (u32)(t_) * 16384u; _Pragma("unroll") for (int q_ = 0; q_ < 4; ++q_) X_[4 + q_] = ldn(rv, vo + q_ * 1024, so_); SB(); }

  const int aid = 2 * h + (role & 1), bA = aid >> 5, sp = aid & 31;
  const int kg = wave >> 2, cs = wave & 3, hh = lane & 31, hf = lane >> 5;
  const rsrc_t rc = mkrs(Ackv + ((size_t)bA * SK + 256 * sp + 32 * kg) * DC + 128 * cs, 4 * 65536 - 32768);
  const u32 co = g * 1024 + n * 16;
  u32x4 st[2][8];
#define A_ISSUE(s_, it_) { _Pragma("unroll") for (int i_ = 0; i_ < 8; ++i_) st[s_][i_] = ldn(rc, co + 4096 * i_, (it_) * 65536); SB(); }

  if (wave >= 4) {
    u32x4 xfB[4], xfA[4], wB[4], wA[4], wk[4];
    {
      if (rq == 0 && tid < 258) __builtin_amdgcn_raw_buffer_store_b32(0u, rs, WS_CNT + (hq * 2 + (tid & 1)) * 128, 0, 16);
      const u32 wo_ = ((128 * sw + (lane >> 1)) * 4096 + hq * 128 + 16 * rq + 8 * (lane & 1)) * 2;
      const rsrc_t r1 = mkrs(Bwq, 1 << 22), r2 = mkrs(Awq, 1 << 22), r3 = mkrs(Awuk, 1 << 22);
#pragma unroll
      for (int t = 0; t < 4; ++t) { wB[t] = ldn(r1, wo_, t * 32 * 8192); wA[t] = ldn(r2, wo_, t * 32 * 8192); }
      const int xo = (n & 1) * 512 + 128 * sw + 8 * g;
#pragma unroll
      for (int t = 0; t < 4; ++t) { xfB[t] = *(const u32x4*)(Bx + xo + 32 * t); xfA[t] = *(const u32x4*)(Ax + xo + 32 * t); }
      const u32 ko_ = ((64 * rq + 16 * sw + ((lane >> 2) & 3)) * 4096 + hq * 128 + 8 * (4 * g + (lane & 3))) * 2;
#pragma unroll
      for (int t = 0; t < 4; ++t) wk[t] = ldn(r3, ko_, t * 4 * 8192);
    }
    SB();
    LBAR();
    u8* wi = lds + L_SX + sw * 8192;
#pragma unroll
    for (int t = 0; t < 4; ++t) { *(u32x4*)(wi + t * 1024 + lane * 16) = wB[t]; *(u32x4*)(wi + 4096 + t * 1024 + lane * 16) = wA[t]; }
    const u8* rp = wi + (8 * g + (n >> 2)) * 32 + 8 * (lane & 3);
    f32x4 dB = {0.f, 0.f, 0.f, 0.f}, dA = dB;
#pragma unroll
    for (int t = 0; t < 4; ++t) {
      { const u32x2 a = tr_read(rp + t * 1024), b_ = tr_read(rp + t * 1024 + 128); dB = mfma16(n < 2 ? xfB[t] : z4, u32x4{a[0], a[1], b_[0], b_[1]}, dB); }
      { const u32x2 a = tr_read(rp + 4096 + t * 1024), b_ = tr_read(rp + 4096 + t * 1024 + 128); dA = mfma16(n < 2 ? xfA[t] : z4, u32x4{a[0], a[1], b_[0], b_[1]}, dA); }
    }
    float* qp = (float*)(lds + L_Q + 256);
    if (lane < 16) { qp[sw * 64 + n] = dB[0]; qp[sw * 64 + 16 + n] = dB[1]; qp[sw * 64 + 32 + n] = dA[0]; qp[sw * 64 + 48 + n] = dA[1]; }
    SVC_BAR(1)
    STAMP_AT(5);
    if (sw == 0 && lane < 16) {
      f32x4 s = {0.f, 0.f, 0.f, 0.f};
#pragma unroll
      for (int w_ = 0; w_ < 4; ++w_) s += *(const f32x4*)&qp[w_ * 64 + 4 * lane];
      gst(rs, WS_QX + ((hq * 8 + rq) * 16 + lane) * 16, cvtpk(s[0], s[1]), cvtpk(s[2], s[3]), ta, tbg);
    }
    if (sw >= 2) {
      const int qo = WS_QX + (((sw == 2 ? h : hq) * 8 + (lane >> 3)) * 16 + (sw - 2) * 8 + (lane & 7)) * 16;
      __builtin_amdgcn_s_sleep(8);
      u32x4 gq = gld(rs, qo);
      GWAIT(gq, qo, 2)
      *(u32x2*)(lds + L_Q + 2048 + ((sw - 2) * 2 + ((lane >> 2) & 1)) * 256 + (lane >> 3) * 32 + (lane & 3) * 8) = u32x2{gq[0], gq[2]};
    }
    SVC_BAR(2)
    STAMP_AT(6);
    const u32x4 qv = *(const u32x4*)(lds + L_Q + 2048 + 512 + ((n >> 2) & 1) * 256 + 16 * (4 * g + (n & 3)));
    const u32x4 qf0 = (n >> 3) == 0 ? qv : z4, qf1 = (n >> 3) == 1 ? qv : z4;
    float* qa = (float*)(lds + L_Q + 4096) + sw * 32;
#pragma unroll
    for (int u = 0; u < 2; ++u) {
      f32x4 d = {0.f, 0.f, 0.f, 0.f};
      d = mfma16(wk[2 * u], qf0, d); d = mfma16(wk[2 * u + 1], qf1, d);
      const float s = quad_diag_sum(d);
      if ((lane & 3) == 0) qa[((lane >> 2) & 1) * 16 + 8 * u + 4 * ((lane >> 3) & 1) + g] = s;
    }
    asm volatile("s_waitcnt lgkmcnt(0)" ::: "memory");
    if (lane < 8) { const f32x4 v = *(const f32x4*)&qa[4 * lane];
      gst(rs, WS_QABS + (((lane >> 2) * 32 + hq) * 128 + 16 * rq + 4 * sw + (lane & 3)) * 16, cvtpk(v[0], v[1]), cvtpk(v[2], v[3]), ta, tbg); }
    SB();
    if (lane == 0) __hip_atomic_fetch_add(&ctl[1], 1u, __ATOMIC_RELAXED, __HIP_MEMORY_SCOPE_WORKGROUP);
    if (isA && !NOA) A_ISSUE(0, 0)
  } else {
    LBAR();
    if (isA && !NOA) A_ISSUE(0, 0)
  }

  if (isA && !NOA) {
    u8* img = lds + L_IMG + wave * 8192;
#define A_STAGE(s_) { _Pragma("unroll") for (int i = 0; i < 8; ++i) *(u32x4*)(img + off_a(4 * i + g, n)) = st[s_][i]; asm volatile("" ::: "memory"); SB(); }
    u32x4 qh[8];
    {
      u8* qhi = lds + L_SX;
      LBAR();
      STAMP_AT(1);
      u32x4 gq[8];
#pragma unroll
      for (int k = 0; k < 8; ++k) gq[k] = gld(rs, WS_QABS + ((bA * 32 + 4 * wave) * 128 + k * 64 + lane) * 16);
      _Pragma("nounroll") for (u32 spin = 0; spin < SPIN_CAP; ++spin) {
        bool ok = true;
#pragma unroll
        for (int k = 0; k < 8; ++k) ok &= gok(gq[k], ta, tbg);
        if (__all(ok)) break;
        __builtin_amdgcn_s_sleep(8);
#pragma unroll
        for (int k = 0; k < 8; ++k) if (!gok(gq[k], ta, tbg)) gq[k] = gld(rs, WS_QABS + ((bA * 32 + 4 * wave) * 128 + k * 64 + lane) * 16);
      }
      SB();
      A_ISSUE(1, 1)
      A_STAGE(0)
      A_ISSUE(0, 2)
#pragma unroll
      for (int k = 0; k < 8; ++k) {
        const int idx = (lane >> 1) & 15, hd = 4 * wave + (k >> 1);
        *(u32x2*)(qhi + ((2 * (k & 1) + (lane >> 5)) * 16 + idx) * 512 + ((hd ^ (idx & 7)) * 16) + (lane & 1) * 8) = u32x2{gq[k][0], gq[k][2]};
      }
      LBAR();
#pragma unroll
      for (int s = 0; s < 8; ++s) qh[s] = *(const u32x4*)(qhi + (cs * 16 + 2 * s + hf) * 512 + ((hh ^ ((2 * s + hf) & 7)) * 16));
      LBAR();
    }
    STAMP_AT(2);
    f32x16 o[4];
#pragma unroll
    for (int c = 0; c < 4; ++c)
#pragma unroll
      for (int r = 0; r < 16; ++r) o[c][r] = 0.f;
    float m = -INFINITY, l = 0.f;
    float* sx = (float*)(lds + L_SX);
    _Pragma("nounroll") for (int it2 = 0; it2 < 4; it2 += 2) {
#pragma unroll
      for (int ip = 0; ip < 2; ++ip) {
        f32x16 sa;
#pragma unroll
        for (int r = 0; r < 16; ++r) sa[r] = 0.f;
#pragma unroll
        for (int ks = 0; ks < 8; ++ks) { const u32x4 a = *(const u32x4*)(img + off_a(hh, 2 * ks + hf)); sa = mfma32(a, qh[ks], sa); }
        float* sb = sx + ip * 8192;
#pragma unroll
        for (int q = 0; q < 4; ++q) *(f32x4*)&sb[wave * 1024 + q * 256 + lane * 4] = f32x4{sa[4 * q], sa[4 * q + 1], sa[4 * q + 2], sa[4 * q + 3]};
        LBAR();
#pragma unroll
        for (int w = 0; w < 4; ++w)
#pragma unroll
          for (int q = 0; q < 4; ++q) { const f32x4 v = *(const f32x4*)&sb[(kg * 4 + w) * 1024 + q * 256 + lane * 4];
            if (w == 0) { sa[4 * q] = v[0]; sa[4 * q + 1] = v[1]; sa[4 * q + 2] = v[2]; sa[4 * q + 3] = v[3]; }
            else { sa[4 * q] += v[0]; sa[4 * q + 1] += v[1]; sa[4 * q + 2] += v[2]; sa[4 * q + 3] += v[3]; }
            if (w == 1 && q == 3) SB(); }
        float mx = sa[0];
#pragma unroll
        for (int r = 1; r < 16; ++r) mx = fmaxf(mx, sa[r]);
        mx = x32_max(mx) * CS;
        if (__any(mx > m)) {
          const float mn = fmaxf(m, mx), al = ex2(m - mn);
          m = mn; l *= al;
#pragma unroll
          for (int c = 0; c < 4; ++c) o[c] *= al;
        }
        u32x4 pf[2];
#pragma unroll
        for (int r = 0; r < 16; r += 2) {
          const float p0 = ex2(fmaf(sa[r], CS, -m)), p1 = ex2(fmaf(sa[r + 1], CS, -m));
          l += p0 + p1; pf[r >> 3][(r >> 1) & 3] = cvtpk(p0, p1);
        }
        const u32 blk = (lane >> 4) & 1, q4 = n >> 2, p4 = lane & 3;
#pragma unroll
        for (int ct = 0; ct < 4; ++ct)
#pragma unroll
          for (int ks = 0; ks < 2; ++ks) {
            const u32x2 lo = tr_read(img + off_a(16 * ks + 4 * hf + q4, 4 * ct + 2 * blk + (p4 >> 1)) + 8 * (p4 & 1));
            const u32x2 hi = tr_read(img + off_a(16 * ks + 8 + 4 * hf + q4, 4 * ct + 2 * blk + (p4 >> 1)) + 8 * (p4 & 1));
            o[ct] = mfma32(u32x4{lo[0], lo[1], hi[0], hi[1]}, pf[ks], o[ct]);
          }
        A_STAGE((ip + 1) & 1)
        A_ISSUE((ip + 1) & 1, it2 + ip + 3)
      }
    }
    STAMP_AT(3);
    {
      l = x32_sum(l);
      LBAR();
      float* ml = (float*)(lds + L_Q + 256);
      float* ob = (float*)(lds + L_IMG);
      u8* om = lds + L_SX;
      if (cs == 0 && lane < 32) *(f32x2*)&ml[(kg * 32 + hh) * 2] = f32x2{m, l};
      if (kg == 1) {
#pragma unroll
        for (int ct = 0; ct < 4; ++ct)
#pragma unroll
          for (int q = 0; q < 4; ++q) *(f32x4*)&ob[(((cs * 4 + ct) * 4 + q) * 64 + lane) * 4] = f32x4{o[ct][4 * q], o[ct][4 * q + 1], o[ct][4 * q + 2], o[ct][4 * q + 3]};
      }
      LBAR();
      if (kg == 0) {
        const f32x2 v1 = *(const f32x2*)&ml[(32 + hh) * 2];
        const float mn = fmaxf(m, v1[0]), a0 = ex2(m - mn), a1 = ex2(v1[0] - mn);
#pragma unroll
        for (int ct = 0; ct < 4; ++ct)
#pragma unroll
          for (int q = 0; q < 4; ++q) {
            const f32x4 o1 = *(const f32x4*)&ob[(((cs * 4 + ct) * 4 + q) * 64 + lane) * 4];
            f32x4 v;
#pragma unroll
            for (int e = 0; e < 4; ++e) v[e] = o[ct][4 * q + e] * a0 + o1[e] * a1;
            const int c4 = 32 * cs + 8 * ct + 2 * q + hf;
            *(u32x2*)(om + hh * 1024 + ((c4 ^ (hh & 15)) * 8)) = u32x2{cvtpk(v[0], v[1]), cvtpk(v[2], v[3])};
          }
        m = mn; l = l * a0 + v1[1] * a1;
      }
      LBAR();
#pragma unroll
      for (int i = 0; i < 4; ++i) {
        const int gi = i * 512 + tid, h2 = gi >> 6, c8 = gi & 63;
        const u32x2 v0 = *(const u32x2*)(om + h2 * 1024 + (((2 * c8) ^ (h2 & 15)) * 8)), v1 = *(const u32x2*)(om + h2 * 1024 + (((2 * c8 + 1) ^ (h2 & 15)) * 8));
        __builtin_amdgcn_raw_buffer_store_b128(u32x4{v0[0], v0[1], v1[0], v1[1]}, rs, WS_PAO + ((h2 * 2 + bA) * 32 + sp) * 1024 + c8 * 16, 0, 16);
      }
      asm volatile("s_waitcnt vmcnt(0)" ::: "memory");
      LBAR();
      if (wave == 0 && lane < 32) gst(rs, WS_PAML + ((hh * 2 + bA) * 32 + sp) * 16, __float_as_uint(m), __float_as_uint(l), ta, tbg);
    }
    STAMP_AT(4);
    SB();
    {
      const int ch = role & 1;
      u32x4 wv[8];
#pragma unroll
      for (int t = 0; t < 8; ++t) wv[t] = ldn(mkrs(Awuv, 1 << 22), ((256 * ch + 32 * wave + 8 * g) * 4096 + h * 128 + 8 * n) * 2, t * 8192);
      SB();
      const int b = wave >> 2, sq = wave & 3;
      const int mo = WS_PAML + ((h * 2 + b) * 32 + hh) * 16;
      u32x4 gm = gld(rs, mo);
      GWAIT(gm, mo, 16)
      u32x4 go[4];
#pragma unroll
      for (int s = 0; s < 4; ++s) go[s] = gld(rs, WS_PAO + ((h * 2 + b) * 32 + 8 * sq + 4 * hf + s) * 1024 + 512 * ch + hh * 16);
      const float ms = __uint_as_float(gm[0]), ls = __uint_as_float(gm[2]);
      const float M = x16_max(row_max(ms));
      float wt = ex2(ms - M);
      const float L = x16_sum(row_sum(wt * ls));
      wt = wt / L;
      f32x4 c0 = {0.f, 0.f, 0.f, 0.f}, c1 = c0;
#pragma unroll
      for (int s = 0; s < 4; ++s) { const float w_ = __shfl(wt, 8 * sq + 4 * hf + s);
        c0 += w_ * f32x4{bflo(go[s][0]), bfhi(go[s][0]), bflo(go[s][1]), bfhi(go[s][1])}; c1 += w_ * f32x4{bflo(go[s][2]), bfhi(go[s][2]), bflo(go[s][3]), bfhi(go[s][3])}; }
      float* cc = (float*)(lds + L_SX);
      *(f32x4*)&cc[((sq * 2 + hf) * 2 + b) * 256 + 8 * hh] = c0; *(f32x4*)&cc[((sq * 2 + hf) * 2 + b) * 256 + 8 * hh + 4] = c1;
      LBAR();
      FRESH_SEL(sel)
      u32 xh[4], xl[4];
#pragma unroll
      for (int k = 0; k < 2; ++k) {
        const int ci = (n >> 3) * 256 + 32 * wave + 8 * g + 4 * k;
        f32x4 v = *(const f32x4*)&cc[ci];
#pragma unroll
        for (int pp = 1; pp < 8; ++pp) v += *(const f32x4*)&cc[pp * 512 + ci];
        xh[2 * k] = cvtpk(v[0], v[1]); xh[2 * k + 1] = cvtpk(v[2], v[3]);
        xl[2 * k] = cvtpk(v[0] - bflo(xh[2 * k]), v[1] - bfhi(xh[2 * k])); xl[2 * k + 1] = cvtpk(v[2] - bflo(xh[2 * k + 1]), v[3] - bfhi(xh[2 * k + 1]));
      }
      f32x4 ca = {0.f, 0.f, 0.f, 0.f};
#pragma unroll
      for (int t = 0; t < 8; ++t) {
        ca = mfma16(place(xh[t >> 1], (t & 1) ? sel.o : sel.e), wv[t], ca);
        ca = mfma16(place(xl[t >> 1], (t & 1) ? sel.o : sel.e), wv[t], ca);
      }
      float* red = (float*)(lds + L_SX + 32768);
      *(f32x4*)&red[wave * 256 + (g >> 1) * 128 + 8 * n + 4 * (g & 1)] = ca;
      LBAR();
      if (wave >= 6) {
        const int pr = tid - 384;
        f32x2 s = {0.f, 0.f};
#pragma unroll
        for (int w = 0; w < 8; ++w) s += *(const f32x2*)&red[w * 256 + 2 * pr];
        gst(rs, WS_CTX + ((h * 2 + ch) * 128 + pr) * 16, __float_as_uint(s[0]), __float_as_uint(s[1]), ta, tbg);
      }
    }
    STAMP_AT(9);
  }

  if (wave >= 4) {
    {
      const int t2 = tid - 256;
      const u32 o_ = ((h * 128 + (t2 >> 3)) * 512 + 64 * role + 8 * (t2 & 7)) * 2; const rsrc_t r1 = mkrs(Bwo, 1 << 22), r2 = mkrs(Awo, 1 << 22);
      u32x4 a[4], c[4];
#pragma unroll
      for (int t = 0; t < 4; ++t) { a[t] = ldn(r1, o_, t * 32768); c[t] = ldn(r2, o_, t * 32768); }
      if (!isA) {
#pragma unroll
        for (int t = 0; t < 4; ++t) { *(u32x4*)(lds + L_IMG + t * 4096 + t2 * 16) = a[t]; *(u32x4*)(lds + L_IMG + 16384 + t * 4096 + t2 * 16) = c[t]; }
      }
      if (sw == 0) {
        const u32 coff = lane ? 0x7fffff00u : (u32)(WS_CNT + (h * 2 + bS) * 128);
        int prod = 0;
        _Pragma("nounroll") for (u32 spin = 0; spin < (SPIN_CAP << 4); ++spin) {
          const int cns = (int)__builtin_amdgcn_readfirstlane(LDS_LD(&ctl[4]));
          const int want = cns + BAHD - bs;
          if (want > prod) {
            if (prod >= TQN - 4) { if (lane == 0) LDS_ST(&tq[prod], 128u); break; }
            const int nc = (want - prod >= 2) ? 2 : 1;
            const int base = T0 + __builtin_amdgcn_readfirstlane(__builtin_amdgcn_raw_ptr_buffer_atomic_add_i32(nc, rs, coff, 0, 0));
            if (lane < nc) LDS_ST(&tq[prod + lane], (u32)(base + lane));
            prod += nc;
            if (base + nc > 128) break;
          } else __builtin_amdgcn_s_sleep(4);
        }
      }
      if (isA) {
#pragma unroll
        for (int t = 0; t < 4; ++t) { *(u32x4*)(lds + L_IMG + t * 4096 + t2 * 16) = a[t]; *(u32x4*)(lds + L_IMG + 16384 + t * 4096 + t2 * 16) = c[t]; }
      }
    }
    LBAR();
  } else {
    u32x4 U[W0T][8];
    if (!isA) {
#pragma unroll
      for (int hp = 0; hp < 2 * W0T; ++hp) {
        if (hp >= BPF) { USE4(U[(hp - BPF) >> 1][((hp - BPF) & 1) * 4 + 3]); SB(); }
        if (hp & 1) B_LV(U[hp >> 1], slot + 3 * (hp >> 1)) else B_LK(U[hp >> 1], slot + 3 * (hp >> 1))
      }
      STAMP_AT(4);
    }
    _Pragma("nounroll") for (u32 spin = 0; spin < (SPIN_CAP << 4); ++spin) { if (LDS_LD(&ctl[1]) >= 4u) break; __builtin_amdgcn_s_sleep(2); }
    asm volatile("" ::: "memory");
    if (!isA) STAMP_AT(1);
    u32x4 qf[4];
    { const u32x4 qv = *(const u32x4*)(lds + L_Q + 2048 + bS * 256 + 16 * (4 * g + (n & 3)));
#pragma unroll
      for (int t = 0; t < 4; ++t) qf[t] = (n >> 2) == t ? qv : z4; }
    u32 msk[4];
#pragma unroll
    for (int r = 0; r < 4; ++r) msk[r] = (n < 8 && (n >> 1) == r) ? ((n & 1) ? 0xffff0000u : 0x0000ffffu) : 0u;
    float m = -1e30f; double l = 0.0, acc[4] = {0.0, 0.0, 0.0, 0.0};
    SB();
#define B_QK(X_) f32x4 d = {0.f, 0.f, 0.f, 0.f}; _Pragma("unroll") for (int t = 0; t < 4; ++t) d = mfma16(X_[t], qf[t], d); asm volatile("" : "+v"(d) :: "memory"); SB();
#define B_PV1(X_, t_) { u32x4 a; const u32 pb = dppu<0x150 + 4 * (t_)>(p2); \
      a[0] = pb & msk[0]; a[1] = pb & msk[1]; a[2] = pb & msk[2]; a[3] = pb & msk[3]; tv = mfma16(a, X_[4 + (t_)], tv); }
#define B_SMPV(X_, valid_) { float sc = quad_diag_sum(d) * CS; if (!(valid_)) sc = -2e30f; \
      const float fl = __builtin_floorf(sc); \
      if (__any(fl >= m)) { const float mn = wave_max(fl) + 1.f; const int e_ = idiff(m, mn); m = mn; \
        l = __builtin_ldexp(l, e_); _Pragma("unroll") for (int r = 0; r < 4; ++r) acc[r] = __builtin_ldexp(acc[r], e_); } \
      const float p = __builtin_ldexpf(ex2(sc - fl), idiff(fl, m)); l += (double)p; const u32 p2 = cvtpk(p, p); \
      f32x4 tv = {0.f, 0.f, 0.f, 0.f}; \
      B_PV1(X_, 0) B_PV1(X_, 1) B_PV1(X_, 2) B_PV1(X_, 3) asm volatile("" : "+v"(tv) :: "memory"); SB(); \
      _Pragma("unroll") for (int r = 0; r < 4; ++r) acc[r] += (double)tv[r]; }
#define B_NEXT(tn_, kk_) u32 tn_ = (u32)(slot + 3 * (kk_)); \
      if (lane == 0) __hip_atomic_fetch_max(&ctl[4], (u32)(kk_), __ATOMIC_RELAXED, __HIP_MEMORY_SCOPE_WORKGROUP); \
      if ((kk_) >= bs) { \
        _Pragma("nounroll") for (u32 spin = 0; spin < (SPIN_CAP << 4); ++spin) { \
          tn_ = __builtin_amdgcn_readfirstlane(LDS_LD(&tq[(kk_) - bs])); \
          if (tn_ != 0xffffffffu) break; \
          __builtin_amdgcn_s_sleep(1); } \
        tn_ = tn_ < 128u ? tn_ : 128u; }
#define B_TS(k_)
    bool va = true, vb = true;
    int k0 = W0T;
    if (!isA) {
      { B_QK(U[0]) B_LK(U[0], slot + 3 * W0T) B_SMPV(U[0], true) B_LV(U[0], slot + 3 * W0T) }
      { B_QK(U[1]) B_LK(U[1], slot + 3 * (W0T + 1)) B_SMPV(U[1], true) B_LV(U[1], slot + 3 * (W0T + 1)) }
#pragma unroll
      for (int pp = 2; pp < W0T; ++pp) { B_QK(U[pp]) B_SMPV(U[pp], true) }
    } else {
      B_LK(U[0], 128) B_LV(U[0], 128) B_LK(U[1], 128) B_LV(U[1], 128)
      va = false; vb = false; k0 = 0;
    }
    bool live = true;
    _Pragma("nounroll") for (int k = k0; k < bs + TQN - 2; k += 2) {
      u32 t0 = 128u, t1 = 128u;
      if (live) { B_NEXT(t0_, k + 2) t0 = t0_; }
      { B_QK(U[0]) B_LK(U[0], t0) B_SMPV(U[0], va) B_LV(U[0], t0) }
      if (va) { B_TS(k) }
      if (t0 < 128u) { B_NEXT(t1_, k + 3) t1 = t1_; }
      { B_QK(U[1]) B_LK(U[1], t1) B_SMPV(U[1], vb) B_LV(U[1], t1) }
      if (vb) { B_TS(k + 1) }
      va = t0 < 128u; vb = t1 < 128u; live = vb;
      if (!va) break;
    }
    STAMP_AT(10);
    float* pm = (float*)(lds + 32768);
    double* pl = (double*)(lds + 32768 + 64);
    double* pacc = (double*)(lds + 32768 + 1024);
    if (lane < 32) { *(f64x2*)&pacc[wave * 128 + 8 * n + 4 * g] = f64x2{acc[0], acc[1]}; *(f64x2*)&pacc[wave * 128 + 8 * n + 4 * g + 2] = f64x2{acc[2], acc[3]}; }
    if ((lane & 3) == 0) pl[wave * 16 + (lane >> 2)] = l;
    if (lane == 0) pm[wave] = m;
    LBAR();
    if (wave == 0) {
      const f32x4 mv = *(const f32x4*)pm;
      const float M = fmaxf(fmaxf(mv[0], mv[1]), fmaxf(mv[2], mv[3]));
      double o0 = 0.0, o1 = 0.0;
#pragma unroll
      for (int w = 0; w < 4; ++w) { const int e = idiff(mv[w], M); const f64x2 a = *(const f64x2*)&pacc[w * 128 + 2 * lane]; o0 += __builtin_ldexp(a[0], e); o1 += __builtin_ldexp(a[1], e); }
      const float h0 = (float)o0, h1 = (float)o1;
      __builtin_amdgcn_raw_buffer_store_b128(u32x4{__float_as_uint(h0), __float_as_uint(h1), cvtpk((float)(o0 - (double)h0), (float)(o1 - (double)h1)), ta},
                                             rs, WS_PB + (((h * 2 + bS) * 4 + slot) * 64 + lane) * 16, 0, 16);
    } else if (wave == 1) {
      const f32x4 mv = *(const f32x4*)pm;
      const float M = fmaxf(fmaxf(mv[0], mv[1]), fmaxf(mv[2], mv[3]));
      double L = __builtin_ldexp(pl[lane], idiff(pm[g], M));
#pragma unroll
      for (int o = 1; o < 64; o <<= 1) L += __shfl_xor(L, o);
      const float Lh = (float)L;
      if (lane < 2) gst(rs, WS_PBML + (((h * 2 + bS) * 4 + slot) * 2 + lane) * 16, __float_as_uint(lane ? (float)(L - (double)Lh) : M), __float_as_uint(lane ? 0.f : Lh), ta, tbg);
    }
    STAMP_AT(11);
  }

  {
    const u8* wi = lds + L_IMG;
    u32* ob = (u32*)(lds + L_Q + 16384);
    if (wave == 4 || wave == 5) {
      const int b = wave & 1;
      const int mo = WS_PBML + ((h * 2 + b) * 8 + (lane & 7)) * 16;
      u32x4 gm = gld(rs, mo), ga[4];
#pragma unroll
      for (int s = 0; s < 4; ++s) ga[s] = gld(rs, WS_PB + (((h * 2 + b) * 4 + s) * 64 + lane) * 16);
      const u32 ok0 = (u32)__ballot(gok(gm, ta, tbg));
      GWAIT(gm, mo, TSLP)
#pragma unroll
      for (int s = 0; s < 4; ++s) if (((ok0 >> (2 * s)) & 3u) != 3u) ga[s] = gld(rs, WS_PB + (((h * 2 + b) * 4 + s) * 64 + lane) * 16);
      _Pragma("nounroll") for (u32 spin = 0; spin < SPIN_CAP; ++spin) {
        bool ok = true;
#pragma unroll
        for (int s = 0; s < 4; ++s) ok &= ga[s][3] == ta;
        if (__all(ok)) break;
        __builtin_amdgcn_s_sleep(1);
#pragma unroll
        for (int s = 0; s < 4; ++s) if (ga[s][3] != ta) ga[s] = gld(rs, WS_PB + (((h * 2 + b) * 4 + s) * 64 + lane) * 16);
      }
      const float ms = (n & 1) ? -3e30f : __uint_as_float(gm[0]);
      const float M = row_max(ms);
      double L = 0.0, o0 = 0.0, o1 = 0.0;
#define H1_ACC(s_) { const int e_ = idiff(dppf<0x150 + 2 * (s_)>(ms), M); \
        L += __builtin_ldexp((double)dppf<0x150 + 2 * (s_)>(__uint_as_float(gm[2])) + (double)dppf<0x151 + 2 * (s_)>(__uint_as_float(gm[0])), e_); \
        o0 += __builtin_ldexp((double)__uint_as_float(ga[s_][0]) + (double)bflo(ga[s_][2]), e_); o1 += __builtin_ldexp((double)__uint_as_float(ga[s_][1]) + (double)bfhi(ga[s_][2]), e_); }
      H1_ACC(0) H1_ACC(1) H1_ACC(2) H1_ACC(3)
      ob[b * 64 + lane] = cvtpk((float)(o0 / L), (float)(o1 / L));
    } else if (wave >= 6) {
      const int pr = tid - 384, c0o = WS_CTX + ((h * 2) * 128 + pr) * 16;
      u32x4 g0 = gld(rs, c0o), g1 = gld(rs, c0o + 2048);
      GWAIT(g0, c0o, 8)
      GWAIT(g1, c0o + 2048, 8)
      ob[128 + pr] = cvtpk(__uint_as_float(g0[0]) + __uint_as_float(g1[0]), __uint_as_float(g0[2]) + __uint_as_float(g1[2]));
    }
    LBAR();
    STAMP_AT(7);
    {
      const int mt = wave >> 2, ct = wave & 3;
      const u8* rp = wi + mt * 16384 + (8 * g + (n >> 2)) * 128 + 32 * ct + 8 * (lane & 3);
      f32x4 y = {0.f, 0.f, 0.f, 0.f};
#pragma unroll
      for (int ks = 0; ks < 4; ++ks) {
        const u32x2 a = tr_read(rp + ks * 4096), bq = tr_read(rp + ks * 4096 + 512);
        const u32x4 of = *(const u32x4*)&ob[mt * 128 + (n & 1) * 64 + 16 * ks + 4 * g];
        y = mfma16(n < 2 ? of : z4, u32x4{a[0], a[1], bq[0], bq[1]}, y);
      }
      if (lane < 16) gst(rs, (mt ? WS_YA : WS_YB) + ((64 * role + 16 * ct + n) * 32 + h) * 16, __float_as_uint(y[0]), __float_as_uint(y[1]), ta, tbg);
    }
    if ((wave & 3) == 0) {
      const int yo = (wave ? WS_YA : WS_YB) + (2 * bx * 32 + lane) * 16;
      u32x4 gy = gld(rs, yo);
      GWAIT(gy, yo, TSLP)
      const float s0 = x16_sum(row_sum(__uint_as_float(gy[0]))), s1 = x16_sum(row_sum(__uint_as_float(gy[2])));
      const float t0 = __shfl(s0, 32), t1 = __shfl(s1, 32);
      if (lane == 0) {
        u16* op = out + (wave ? 0 : 512) + 2 * bx;
        *(u32*)op = cvtpk(s0, t0); *(u32*)(op + 1024) = cvtpk(s1, t1);
      }
    }
    if (bx == 0) {
      LBAR();
      if (tid == 0) __builtin_amdgcn_raw_buffer_store_b128(u32x4{enext, e1n, 0u, 0u}, rs, WS_EPOCH, 0, 16);
    }
    STAMP_AT(8);
    STAMP_FLUSH(12);
  }
}

extern "C" void kernel_launch(void* const* d_in, const int* in_sizes, int n_in, void* d_out, int out_size, void* d_ws, size_t ws_size, hipStream_t stream) {
  constexpr int LDSB = 163840;
  (void)hipFuncSetAttribute((const void*)fused, hipFuncAttributeMaxDynamicSharedMemorySize, LDSB);
  fused<<<256, 512, LDSB, stream>>>((const u16*)d_in[0], (const u16*)d_in[1], (const u16*)d_in[2], (const u16*)d_in[3], (const u16*)d_in[4], (const u16*)d_in[5],
                                    (const u16*)d_in[6], (const u16*)d_in[7], (const u16*)d_in[8], (const u16*)d_in[9], (const u16*)d_in[10], (u16*)d_out, (unsigned char*)d_ws);
}
```

```cpp
#include <hip/hip_runtime.h>
#include <stdint.h>

typedef unsigned int u32;
typedef unsigned short u16;
typedef unsigned char u8;
typedef __attribute__((ext_vector_type(4))) unsigned u32x4;
typedef __attribute__((ext_vector_type(2))) unsigned u32x2;
typedef __attribute__((ext_vector_type(4))) float f32x4;
typedef __attribute__((ext_vector_type(2))) float f32x2;
typedef __attribute__((ext_vector_type(2))) double f64x2;
typedef __attribute__((ext_vector_type(16))) float f32x16;
typedef __attribute__((ext_vector_type(8))) __bf16 bf16x8;
typedef __attribute__((ext_vector_type(2))) __bf16 bf16x2;
typedef __attribute__((ext_vector_type(4))) short s16x4;
typedef __attribute__((address_space(3))) s16x4* lds_s16x4p;
typedef __amdgpu_buffer_rsrc_t rsrc_t;
#define DI __device__ __forceinline__

#define STAMP 0
#define BSTAT 28
#define BAHD 3
#define BW0 32
#define BPF 2
#define NOA 0
#define PFCODE 1
#define PFTLB 0
#define TSLP 4

constexpr int TQN = 64;
constexpr int SK = 8192, DC = 512;
constexpr float CS = 0.08838834764831845f * 1.4426950408889634f;

DI f32x4 mfma16(u32x4 a, u32x4 b, f32x4 c) {
  return __builtin_amdgcn_mfma_f32_16x16x32_bf16(__builtin_bit_cast(bf16x8, a), __builtin_bit_cast(bf16x8, b), c, 0, 0, 0);
}
DI f32x16 mfma32(u32x4 a, u32x4 b, f32x16 c) {
  return __builtin_amdgcn_mfma_f32_32x32x16_bf16(__builtin_bit_cast(bf16x8, a), __builtin_bit_cast(bf16x8, b), c, 0, 0, 0);
}
DI u32 cvtpk(float lo, float hi) { return __builtin_bit_cast(u32, __builtin_convertvector(f32x2{lo, hi}, bf16x2)); }
DI float bflo(u32 h) { return __uint_as_float(h << 16); }
DI float bfhi(u32 h) { return __uint_as_float(h & 0xffff0000u); }
template <int CTRL> DI u32 dppu(u32 x) { return (u32)__builtin_amdgcn_mov_dpp((int)x, CTRL, 0xf, 0xf, true); }
template <int CTRL> DI float dppf(float x) { return __uint_as_float(dppu<CTRL>(__float_as_uint(x))); }
DI rsrc_t mkrs(const void* p, u32 bytes) { return __builtin_amdgcn_make_buffer_rsrc((void*)p, 0, bytes, 0x00020000); }
DI u32x4 ldn(rsrc_t r, u32 voff, u32 soff) { return __builtin_amdgcn_raw_buffer_load_b128(r, voff, soff, 2); }
DI float x16_sum(float x) { auto s = __builtin_amdgcn_permlane16_swap(__float_as_uint(x), __float_as_uint(x), false, false); return __uint_as_float(s[0]) + __uint_as_float(s[1]); }
DI float x32_sum(float x) { auto t = __builtin_amdgcn_permlane32_swap(__float_as_uint(x), __float_as_uint(x), false, false); return __uint_as_float(t[0]) + __uint_as_float(t[1]); }
DI float x16_max(float x) { auto s = __builtin_amdgcn_permlane16_swap(__float_as_uint(x), __float_as_uint(x), false, false); return fmaxf(__uint_as_float(s[0]), __uint_as_float(s[1])); }
DI float x32_max(float x) { auto t = __builtin_amdgcn_permlane32_swap(__float_as_uint(x), __float_as_uint(x), false, false); return fmaxf(__uint_as_float(t[0]), __uint_as_float(t[1])); }
DI float row_max(float x) { x = fmaxf(x, dppf<0xB1>(x)); x = fmaxf(x, dppf<0x4E>(x)); x = fmaxf(x, dppf<0x141>(x)); return fmaxf(x, dppf<0x128>(x)); }
DI float row_sum(float x) { x += dppf<0xB1>(x); x += dppf<0x4E>(x); x += dppf<0x141>(x); return x + dppf<0x128>(x); }
DI float wave_max(float x) { return x32_max(x16_max(row_max(x))); }
DI float quad_diag_sum(f32x4 d) { float s = dppf<0x00>(d[0]); s += dppf<0x55>(d[1]); s += dppf<0xAA>(d[2]); s += dppf<0xFF>(d[3]); return s; }
DI float ex2(float x) { return __builtin_amdgcn_exp2f(x); }
DI int idiff(float a, float b) { return (int)fmaxf(a - b, -4096.f); }
#define LBAR() do { asm volatile("s_waitcnt lgkmcnt(0)" ::: "memory"); __builtin_amdgcn_s_barrier(); asm volatile("" ::: "memory"); } while (0)
#define SB() __builtin_amdgcn_sched_barrier(0)
#define USE4(x_) asm volatile("" :: "v"(x_))
#define LDS_LD(p_) __hip_atomic_load(p_, __ATOMIC_RELAXED, __HIP_MEMORY_SCOPE_WORKGROUP)
#define LDS_ST(p_, v_) __hip_atomic_store(p_, v_, __ATOMIC_RELAXED, __HIP_MEMORY_SCOPE_WORKGROUP)

constexpr int WS_EPOCH = 0;
constexpr int WS_QX = 4096;
constexpr int WS_QABS = WS_QX + 32 * 2048;
constexpr int WS_PAO = WS_QABS + 2 * 32 * 2048;
constexpr int WS_PAML = WS_PAO + 32 * 64 * 1024;
constexpr int WS_PB = WS_PAML + 32 * 64 * 16;
constexpr int WS_PBML = WS_PB + 32 * 8 * 1024;
constexpr int WS_CNT = WS_PBML + 32 * 8 * 32;
constexpr int WS_CTX = WS_CNT + 64 * 128;
constexpr int WS_YA = WS_CTX + 32 * 4096;
constexpr int WS_YB = WS_YA + 512 * 512;
constexpr int WS_END = WS_YB + 512 * 512;
constexpr int WS_STAMP = 8 << 20;
constexpr u32 SPIN_CAP = 1u << 16;

DI u32x4 gld(rsrc_t rs, int off) { asm volatile("" ::: "memory"); return __builtin_amdgcn_raw_buffer_load_b128(rs, off, 0, 16); }
DI void gst(rsrc_t rs, int off, u32 v0, u32 v1, u32 ta, u32 tb) { __builtin_amdgcn_raw_buffer_store_b128(u32x4{v0, ta, v1, tb}, rs, off, 0, 16); }
DI bool gok(u32x4 g, u32 ta, u32 tb) { return g[1] == ta && g[3] == tb; }
#define GWAIT(g_, off_, slp_) { _Pragma("nounroll") for (u32 spin = 0; spin < SPIN_CAP; ++spin) { \
    const bool ok_ = gok(g_, ta, tbg); if (__all(ok_)) break; __builtin_amdgcn_s_sleep(slp_); if (!ok_) g_ = gld(rs, off_); } }

#define STAMP_AT(k_) do {} while (0)
#define STAMP_FLUSH(n_) do {} while (0)

struct Sel { u32 e[4], o[4]; };
DI Sel make_sel(int lane) {
  Sel s; const int slot = lane & 7, r0 = slot >> 1, half = slot & 1;
#pragma unroll
  for (int r = 0; r < 4; ++r) {
    s.e[r] = (r == r0) ? (half ? 0x01000c0cu : 0x0c0c0100u) : 0x0c0c0c0cu;
    s.o[r] = (r == r0) ? (half ? 0x03020c0cu : 0x0c0c0302u) : 0x0c0c0c0cu;
  }
  return s;
}
DI u32x4 place(u32 src, const u32 (&sel)[4]) {
  u32x4 a;
#pragma unroll
  for (int r = 0; r < 4; ++r) a[r] = __builtin_amdgcn_perm(0u, src, sel[r]);
  return a;
}
#define FRESH_SEL(name_) int name_##_l = lane; asm volatile("" : "+v"(name_##_l)); const Sel name_ = make_sel(name_##_l);
DI u32 off_a(u32 row, u32 ch) { return 2048u * (row >> 3) + 512u * (ch >> 2) + 64u * (row & 7) + 16u * ((ch & 3) ^ ((row >> 2) & 3)); }
DI u32x2 tr_read(const u8* p) { return __builtin_bit_cast(u32x2, __builtin_amdgcn_ds_read_tr16_b64_v4i16((lds_s16x4p)p)); }

constexpr int L_IMG = 0;
constexpr int L_SX = 65536;
constexpr int L_Q = 131072;

#define SVC_BAR(k_) { asm volatile("s_waitcnt lgkmcnt(0)" ::: "memory"); if (lane == 0) __hip_atomic_fetch_add(&ctl[0], 1u, __ATOMIC_RELAXED, __HIP_MEMORY_SCOPE_WORKGROUP); \
    _Pragma("nounroll") for (u32 spin = 0; spin < (SPIN_CAP << 4); ++spin) { if (LDS_LD(&ctl[0]) >= 4u * (k_)) break; __builtin_amdgcn_s_sleep(1); } \
    asm volatile("" ::: "memory"); }

__global__ void __launch_bounds__(512, 2) fused(const u16* __restrict__ Ax, const u16* __restrict__ Ackv, const u16* __restrict__ Awq, const u16* __restrict__ Awuk,
                                                const u16* __restrict__ Awuv, const u16* __restrict__ Awo, const u16* __restrict__ Bx, const u16* __restrict__ Kc,
                                                const u16* __restrict__ Vc, const u16* __restrict__ Bwq, const u16* __restrict__ Bwo, u16* __restrict__ out, unsigned char* ws) {
  extern __shared__ __attribute__((aligned(16))) unsigned char lds[];
  const int tid = threadIdx.x, lane = tid & 63, wave = __builtin_amdgcn_readfirstlane(tid >> 6);
  const int bx = blockIdx.x, x8 = bx & 7, jj = bx >> 3;
  const int h = jj, role = (x8 + jj) & 7;
  const int hq = 4 * x8 + (jj >> 3), rq = jj & 7;
  const int sw = wave - 4;
  const int g = lane >> 4, n = lane & 15;
  const bool isA = role >= 6;
  const rsrc_t rs = mkrs(ws, WS_END);
  STAMP_AT(0);
  const u32x2 ep = *(const __attribute__((address_space(4))) u32x2*)(ws + WS_EPOCH);
  const u32 enext = ep[0] + 1u, e1n = ep[1];
  const u32 ta = (enext * 0x9E3779B1u) ^ 0x7F4A7C15u, tbg = ((enext ^ e1n) * 0x85EBCA77u) ^ (e1n >> 7) ^ 0x2545F491u;
  const u32x4 z4 = {0u, 0u, 0u, 0u};
  u32* ctl = (u32*)(lds + L_Q);
  u32* tq = (u32*)(lds + L_Q + 8192);
  if (tid < 8) ctl[tid] = 0u;
  if (tid < TQN) tq[tid] = 0xffffffffu;
  constexpr int T0 = 3 * BSTAT;
  constexpr int W0T = BW0 / 8;
  const int bs = isA ? 2 : BSTAT;

  const int bS = (0x66 >> role) & 1, slot = role >> 1;
  const rsrc_t rk = mkrs((const u8*)Kc + ((size_t)(bS * 32 + h) << 21), 1 << 21), rv = mkrs((const u8*)Vc + ((size_t)(bS * 32 + h) << 21), 1 << 21);
  const u32 ko = (((lane >> 2) & 3) * 16 + 4 * g + (lane & 3)) * 16 + (wave & 3) * 4096, vo = lane * 16 + (wave & 3) * 4096;
#define B_LK(X_, t_) { const u32 so_ = (u32)(t_) * 16384u; _Pragma("unroll") for (int q_ = 0; q_ < 4; ++q_) X_[q_] = ldn(rk, ko + q_ * 1024, so_); SB(); }
#define B_LV(X_, t_) { const u32 so_ = (u32)(t_) * 16384u; _Pragma("unroll") for (int q_ = 0; q_ < 4; ++q_) X_[4 + q_] = ldn(rv, vo + q_ * 1024, so_); SB(); }

  const int aid = 2 * h + (role & 1), bA = aid >> 5, sp = aid & 31;
  const int kg = wave >> 2, cs = wave & 3, hh = lane & 31, hf = lane >> 5;
  const rsrc_t rc = mkrs(Ackv + ((size_t)bA * SK + 256 * sp + 32 * kg) * DC + 128 * cs, 4 * 65536 - 32768);
  const u32 co = g * 1024 + n * 16;
  u32x4 st[2][8];
#define A_ISSUE(s_, it_) { _Pragma("unroll") for (int i_ = 0; i_ < 8; ++i_) st[s_][i_] = ldn(rc, co + 4096 * i_, (it_) * 65536); SB(); }

  if (wave == 3) {
    unsigned long long pc; asm volatile("s_getpc_b64 %0" : "=s"(pc));
    const u32x4 cv = __builtin_amdgcn_raw_buffer_load_b128(mkrs((const void*)(pc & ~15ull), 23552), lane * 16, jj * 1024, 0);
    USE4(cv);
  }
  if (wave >= 4) {
    u32x4 xfB[4], xfA[4], wB[4], wA[4], wk[4];
    {
      if (rq == 0 && tid < 258) __builtin_amdgcn_raw_buffer_store_b32(0u, rs, WS_CNT + (hq * 2 + (tid & 1)) * 128, 0, 16);
      const u32 wo_ = ((128 * sw + (lane >> 1)) * 4096 + hq * 128 + 16 * rq + 8 * (lane & 1)) * 2;
      const rsrc_t r1 = mkrs(Bwq, 1 << 22), r2 = mkrs(Awq, 1 << 22), r3 = mkrs(Awuk, 1 << 22);
#pragma unroll
      for (int t = 0; t < 4; ++t) { wB[t] = ldn(r1, wo_, t * 32 * 8192); wA[t] = ldn(r2, wo_, t * 32 * 8192); }
      const int xo = (n & 1) * 512 + 128 * sw + 8 * g;
#pragma unroll
      for (int t = 0; t < 4; ++t) { xfB[t] = *(const u32x4*)(Bx + xo + 32 * t); xfA[t] = *(const u32x4*)(Ax + xo + 32 * t); }
      const u32 ko_ = ((64 * rq + 16 * sw + ((lane >> 2) & 3)) * 4096 + hq * 128 + 8 * (4 * g + (lane & 3))) * 2;
#pragma unroll
      for (int t = 0; t < 4; ++t) wk[t] = ldn(r3, ko_, t * 4 * 8192);
    }
    SB();
    LBAR();
    u8* wi = lds + L_SX + sw * 8192;
#pragma unroll
    for (int t = 0; t < 4; ++t) { *(u32x4*)(wi + t * 1024 + lane * 16) = wB[t]; *(u32x4*)(wi + 4096 + t * 1024 + lane * 16) = wA[t]; }
    const u8* rp = wi + (8 * g + (n >> 2)) * 32 + 8 * (lane & 3);
    f32x4 dB = {0.f, 0.f, 0.f, 0.f}, dA = dB;
#pragma unroll
    for (int t = 0; t < 4; ++t) {
      { const u32x2 a = tr_read(rp + t * 1024), b_ = tr_read(rp + t * 1024 + 128); dB = mfma16(n < 2 ? xfB[t] : z4, u32x4{a[0], a[1], b_[0], b_[1]}, dB); }
      { const u32x2 a = tr_read(rp + 4096 + t * 1024), b_ = tr_read(rp + 4096 + t * 1024 + 128); dA = mfma16(n < 2 ? xfA[t] : z4, u32x4{a[0], a[1], b_[0], b_[1]}, dA); }
    }
    float* qp = (float*)(lds + L_Q + 256);
    if (lane < 16) { qp[sw * 64 + n] = dB[0]; qp[sw * 64 + 16 + n] = dB[1]; qp[sw * 64 + 32 + n] = dA[0]; qp[sw * 64 + 48 + n] = dA[1]; }
    SVC_BAR(1)
    STAMP_AT(5);
    if (sw == 0 && lane < 16) {
      f32x4 s = {0.f, 0.f, 0.f, 0.f};
#pragma unroll
      for (int w_ = 0; w_ < 4; ++w_) s += *(const f32x4*)&qp[w_ * 64 + 4 * lane];
      gst(rs, WS_QX + ((hq * 8 + rq) * 16 + lane) * 16, cvtpk(s[0], s[1]), cvtpk(s[2], s[3]), ta, tbg);
    }
    if (sw >= 2) {
      const int qo = WS_QX + (((sw == 2 ? h : hq) * 8 + (lane >> 3)) * 16 + (sw - 2) * 8 + (lane & 7)) * 16;
      __builtin_amdgcn_s_sleep(8);
      u32x4 gq = gld(rs, qo);
      GWAIT(gq, qo, 2)
      *(u32x2*)(lds + L_Q + 2048 + ((sw - 2) * 2 + ((lane >> 2) & 1)) * 256 + (lane >> 3) * 32 + (lane & 3) * 8) = u32x2{gq[0], gq[2]};
    }
    SVC_BAR(2)
    STAMP_AT(6);
    const u32x4 qv = *(const u32x4*)(lds + L_Q + 2048 + 512 + ((n >> 2) & 1) * 256 + 16 * (4 * g + (n & 3)));
    const u32x4 qf0 = (n >> 3) == 0 ? qv : z4, qf1 = (n >> 3) == 1 ? qv : z4;
    float* qa = (float*)(lds + L_Q + 4096) + sw * 32;
#pragma unroll
    for (int u = 0; u < 2; ++u) {
      f32x4 d = {0.f, 0.f, 0.f, 0.f};
      d = mfma16(wk[2 * u], qf0, d); d = mfma16(wk[2 * u + 1], qf1, d);
      const float s = quad_diag_sum(d);
      if ((lane & 3) == 0) qa[((lane >> 2) & 1) * 16 + 8 * u + 4 * ((lane >> 3) & 1) + g] = s;
    }
    asm volatile("s_waitcnt lgkmcnt(0)" ::: "memory");
    if (lane < 8) { const f32x4 v = *(const f32x4*)&qa[4 * lane];
      gst(rs, WS_QABS + (((lane >> 2) * 32 + hq) * 128 + 16 * rq + 4 * sw + (lane & 3)) * 16, cvtpk(v[0], v[1]), cvtpk(v[2], v[3]), ta, tbg); }
    SB();
    if (lane == 0) __hip_atomic_fetch_add(&ctl[1], 1u, __ATOMIC_RELAXED, __HIP_MEMORY_SCOPE_WORKGROUP);
    if (isA && !NOA) { A_ISSUE(0, 0) A_ISSUE(1, 1) }
  } else {
    LBAR();
    if (isA && !NOA) { A_ISSUE(0, 0) USE4(st[0][7]); SB(); A_ISSUE(1, 1) USE4(st[1][7]); SB(); }
  }

  if (isA && !NOA) {
    u8* img = lds + L_IMG + wave * 8192;
#define A_STAGE(s_) { _Pragma("unroll") for (int i = 0; i < 8; ++i) *(u32x4*)(img + off_a(4 * i + g, n)) = st[s_][i]; asm volatile("" ::: "memory"); SB(); }
    u32x4 qh[8];
    {
      u8* qhi = lds + L_SX;
      LBAR();
      STAMP_AT(1);
      u32x4 gq[8];
#pragma unroll
      for (int k = 0; k < 8; ++k) gq[k] = gld(rs, WS_QABS + ((bA * 32 + 4 * wave) * 128 + k * 64 + lane) * 16);
      _Pragma("nounroll") for (u32 spin = 0; spin < SPIN_CAP; ++spin) {
        bool ok = true;
#pragma unroll
        for (int k = 0; k < 8; ++k) ok &= gok(gq[k], ta, tbg);
        if (__all(ok)) break;
        __builtin_amdgcn_s_sleep(8);
#pragma unroll
        for (int k = 0; k < 8; ++k) if (!gok(gq[k], ta, tbg)) gq[k] = gld(rs, WS_QABS + ((bA * 32 + 4 * wave) * 128 + k * 64 + lane) * 16);
      }
      SB();
      A_STAGE(0)
      A_ISSUE(0, 2)
#pragma unroll
      for (int k = 0; k < 8; ++k) {
        const int idx = (lane >> 1) & 15, hd = 4 * wave + (k >> 1);
        *(u32x2*)(qhi + ((2 * (k & 1) + (lane >> 5)) * 16 + idx) * 512 + ((hd ^ (idx & 7)) * 16) + (lane & 1) * 8) = u32x2{gq[k][0], gq[k][2]};
      }
      LBAR();
#pragma unroll
      for (int s = 0; s < 8; ++s) qh[s] = *(const u32x4*)(qhi + (cs * 16 + 2 * s + hf) * 512 + ((hh ^ ((2 * s + hf) & 7)) * 16));
      LBAR();
    }
    STAMP_AT(2);
    f32x16 o[4];
#pragma unroll
    for (int c = 0; c < 4; ++c)
#pragma unroll
      for (int r = 0; r < 16; ++r) o[c][r] = 0.f;
    float m = -INFINITY, l = 0.f;
    float* sx = (float*)(lds + L_SX);
    _Pragma("nounroll") for (int it2 = 0; it2 < 4; it2 += 2) {
#pragma unroll
      for (int ip = 0; ip < 2; ++ip) {
        f32x16 sa;
#pragma unroll
        for (int r = 0; r < 16; ++r) sa[r] = 0.f;
#pragma unroll
        for (int ks = 0; ks < 8; ++ks) { const u32x4 a = *(const u32x4*)(img + off_a(hh, 2 * ks + hf)); sa = mfma32(a, qh[ks], sa); }
        float* sb = sx + ip * 8192;
#pragma unroll
        for (int q = 0; q < 4; ++q) *(f32x4*)&sb[wave * 1024 + q * 256 + lane * 4] = f32x4{sa[4 * q], sa[4 * q + 1], sa[4 * q + 2], sa[4 * q + 3]};
        LBAR();
#pragma unroll
        for (int w = 0; w < 4; ++w)
#pragma unroll
          for (int q = 0; q < 4; ++q) { const f32x4 v = *(const f32x4*)&sb[(kg * 4 + w) * 1024 + q * 256 + lane * 4];
            if (w == 0) { sa[4 * q] = v[0]; sa[4 * q + 1] = v[1]; sa[4 * q + 2] = v[2]; sa[4 * q + 3] = v[3]; }
            else { sa[4 * q] += v[0]; sa[4 * q + 1] += v[1]; sa[4 * q + 2] += v[2]; sa[4 * q + 3] += v[3]; }
            if (w == 1 && q == 3) SB(); }
        float mx = sa[0];
#pragma unroll
        for (int r = 1; r < 16; ++r) mx = fmaxf(mx, sa[r]);
        mx = x32_max(mx) * CS;
        if (__any(mx > m)) {
          const float mn = fmaxf(m, mx), al = ex2(m - mn);
          m = mn; l *= al;
#pragma unroll
          for (int c = 0; c < 4; ++c) o[c] *= al;
        }
        u32x4 pf[2];
#pragma unroll
        for (int r = 0; r < 16; r += 2) {
          const float p0 = ex2(fmaf(sa[r], CS, -m)), p1 = ex2(fmaf(sa[r + 1], CS, -m));
          l += p0 + p1; pf[r >> 3][(r >> 1) & 3] = cvtpk(p0, p1);
        }
        const u32 blk = (lane >> 4) & 1, q4 = n >> 2, p4 = lane & 3;
#pragma unroll
        for (int ct = 0; ct < 4; ++ct)
#pragma unroll
          for (int ks = 0; ks < 2; ++ks) {
            const u32x2 lo = tr_read(img + off_a(16 * ks + 4 * hf + q4, 4 * ct + 2 * blk + (p4 >> 1)) + 8 * (p4 & 1));
            const u32x2 hi = tr_read(img + off_a(16 * ks + 8 + 4 * hf + q4, 4 * ct + 2 * blk + (p4 >> 1)) + 8 * (p4 & 1));
            o[ct] = mfma32(u32x4{lo[0], lo[1], hi[0], hi[1]}, pf[ks], o[ct]);
          }
        A_STAGE((ip + 1) & 1)
        A_ISSUE((ip + 1) & 1, it2 + ip + 3)
      }
    }
    STAMP_AT(3);
    {
      l = x32_sum(l);
      LBAR();
      float* ml = (float*)(lds + L_Q + 256);
      float* ob = (float*)(lds + L_IMG);
      u8* om = lds + L_SX;
      if (cs == 0 && lane < 32) *(f32x2*)&ml[(kg * 32 + hh) * 2] = f32x2{m, l};
      if (kg == 1) {
#pragma unroll
        for (int ct = 0; ct < 4; ++ct)
#pragma unroll
          for (int q = 0; q < 4; ++q) *(f32x4*)&ob[(((cs * 4 + ct) * 4 + q) * 64 + lane) * 4] = f32x4{o[ct][4 * q], o[ct][4 * q + 1], o[ct][4 * q + 2], o[ct][4 * q + 3]};
      }
      LBAR();
      if (kg == 0) {
        const f32x2 v1 = *(const f32x2*)&ml[(32 + hh) * 2];
        const float mn = fmaxf(m, v1[0]), a0 = ex2(m - mn), a1 = ex2(v1[0] - mn);
#pragma unroll
        for (int ct = 0; ct < 4; ++ct)
#pragma unroll
          for (int q = 0; q < 4; ++q) {
            const f32x4 o1 = *(const f32x4*)&ob[(((cs * 4 + ct) * 4 + q) * 64 + lane) * 4];
            f32x4 v;
#pragma unroll
            for (int e = 0; e < 4; ++e) v[e] = o[ct][4 * q + e] * a0 + o1[e] * a1;
            const int c4 = 32 * cs + 8 * ct + 2 * q + hf;
            *(u32x2*)(om + hh * 1024 + ((c4 ^ (hh & 15)) * 8)) = u32x2{cvtpk(v[0], v[1]), cvtpk(v[2], v[3])};
          }
        m = mn; l = l * a0 + v1[1] * a1;
      }
      LBAR();
#pragma unroll
      for (int i = 0; i < 4; ++i) {
        const int gi = i * 512 + tid, h2 = gi >> 6, c8 = gi & 63;
        const u32x2 v0 = *(const u32x2*)(om + h2 * 1024 + (((2 * c8) ^ (h2 & 15)) * 8)), v1 = *(const u32x2*)(om + h2 * 1024 + (((2 * c8 + 1) ^ (h2 & 15)) * 8));
        __builtin_amdgcn_raw_buffer_store_b128(u32x4{v0[0], v0[1], v1[0], v1[1]}, rs, WS_PAO + ((h2 * 2 + bA) * 32 + sp) * 1024 + c8 * 16, 0, 16);
      }
      asm volatile("s_waitcnt vmcnt(0)" ::: "memory");
      LBAR();
      if (wave == 0 && lane < 32) gst(rs, WS_PAML + ((hh * 2 + bA) * 32 + sp) * 16, __float_as_uint(m), __float_as_uint(l), ta, tbg);
    }
    STAMP_AT(4);
    SB();
    {
      const int ch = role & 1;
      u32x4 wv[8];
#pragma unroll
      for (int t = 0; t < 8; ++t) wv[t] = ldn(mkrs(Awuv, 1 << 22), ((256 * ch + 32 * wave + 8 * g) * 4096 + h * 128 + 8 * n) * 2, t * 8192);
      SB();
      const int b = wave >> 2, sq = wave & 3;
      const int mo = WS_PAML + ((h * 2 + b) * 32 + hh) * 16;
      u32x4 gm = gld(rs, mo);
      GWAIT(gm, mo, 16)
      u32x4 go[4];
#pragma unroll
      for (int s = 0; s < 4; ++s) go[s] = gld(rs, WS_PAO + ((h * 2 + b) * 32 + 8 * sq + 4 * hf + s) * 1024 + 512 * ch + hh * 16);
      const float ms = __uint_as_float(gm[0]), ls = __uint_as_float(gm[2]);
      const float M = x16_max(row_max(ms));
      float wt = ex2(ms - M);
      const float L = x16_sum(row_sum(wt * ls));
      wt = wt / L;
      f32x4 c0 = {0.f, 0.f, 0.f, 0.f}, c1 = c0;
#pragma unroll
      for (int s = 0; s < 4; ++s) { const float w_ = __shfl(wt, 8 * sq + 4 * hf + s);
        c0 += w_ * f32x4{bflo(go[s][0]), bfhi(go[s][0]), bflo(go[s][1]), bfhi(go[s][1])}; c1 += w_ * f32x4{bflo(go[s][2]), bfhi(go[s][2]), bflo(go[s][3]), bfhi(go[s][3])}; }
      float* cc = (float*)(lds + L_SX);
      *(f32x4*)&cc[((sq * 2 + hf) * 2 + b) * 256 + 8 * hh] = c0; *(f32x4*)&cc[((sq * 2 + hf) * 2 + b) * 256 + 8 * hh + 4] = c1;
      LBAR();
      FRESH_SEL(sel)
      u32 xh[4], xl[4];
#pragma unroll
      for (int k = 0; k < 2; ++k) {
        const int ci = (n >> 3) * 256 + 32 * wave + 8 * g + 4 * k;
        f32x4 v = *(const f32x4*)&cc[ci];
#pragma unroll
        for (int pp = 1; pp < 8; ++pp) v += *(const f32x4*)&cc[pp * 512 + ci];
        xh[2 * k] = cvtpk(v[0], v[1]); xh[2 * k + 1] = cvtpk(v[2], v[3]);
        xl[2 * k] = cvtpk(v[0] - bflo(xh[2 * k]), v[1] - bfhi(xh[2 * k])); xl[2 * k + 1] = cvtpk(v[2] - bflo(xh[2 * k + 1]), v[3] - bfhi(xh[2 * k + 1]));
      }
      f32x4 ca = {0.f, 0.f, 0.f, 0.f};
#pragma unroll
      for (int t = 0; t < 8; ++t) {
        ca = mfma16(place(xh[t >> 1], (t & 1) ? sel.o : sel.e), wv[t], ca);
        ca = mfma16(place(xl[t >> 1], (t & 1) ? sel.o : sel.e), wv[t], ca);
      }
      float* red = (float*)(lds + L_SX + 32768);
      *(f32x4*)&red[wave * 256 + (g >> 1) * 128 + 8 * n + 4 * (g & 1)] = ca;
      LBAR();
      if (wave >= 6) {
        const int pr = tid - 384;
        f32x2 s = {0.f, 0.f};
#pragma unroll
        for (int w = 0; w < 8; ++w) s += *(const f32x2*)&red[w * 256 + 2 * pr];
        gst(rs, WS_CTX + ((h * 2 + ch) * 128 + pr) * 16, __float_as_uint(s[0]), __float_as_uint(s[1]), ta, tbg);
      }
    }
    LBAR();
    STAMP_AT(9);
  }

  if (wave >= 4) {
    if (sw == 0) {
      const u32 coff = lane ? 0x7fffff00u : (u32)(WS_CNT + (h * 2 + bS) * 128);
      int prod = 0;
      _Pragma("nounroll") for (u32 spin = 0; spin < (SPIN_CAP << 4); ++spin) {
        const int cns = (int)__builtin_amdgcn_readfirstlane(LDS_LD(&ctl[4]));
        const int want = cns + BAHD - bs;
        if (want > prod) {
          if (prod >= TQN - 4) { if (lane == 0) LDS_ST(&tq[prod], 128u); break; }
          const int nc = (want - prod >= 2) ? 2 : 1;
          const int base = T0 + __builtin_amdgcn_readfirstlane(__builtin_amdgcn_raw_ptr_buffer_atomic_add_i32(nc, rs, coff, 0, 0));
          if (lane < nc) LDS_ST(&tq[prod + lane], (u32)(base + lane));
          prod += nc;
          if (base + nc > 128) break;
        } else __builtin_amdgcn_s_sleep(4);
      }
    } else if (isA && sw < 3) {
      const rsrc_t rw = mkrs((role == 6 ? Bwo : Awo) + (size_t)h * 65536, 131072);
      _Pragma("nounroll") for (int c = 0; c < 8; ++c) {
        u32x4 a[8];
        const int r0 = 16 * c + 8 * (sw - 1);
#pragma unroll
        for (int t = 0; t < 8; ++t) a[t] = ldn(rw, lane * 16, (r0 + t) * 1024);
#pragma unroll
        for (int t = 0; t < 8; ++t) { const int r = r0 + t; *(u32x4*)(lds + r * 1024 + (((lane >> 1) ^ ((r & 3) | ((r >> 1) & 4))) * 32) + (lane & 1) * 16) = a[t]; }
      }
    }
    LBAR();
  } else {
    u32x4 U[W0T][8];
    if (!isA) {
#pragma unroll
      for (int hp = 0; hp < 2 * W0T; ++hp) {
        if (hp >= BPF) { USE4(U[(hp - BPF) >> 1][((hp - BPF) & 1) * 4 + 3]); SB(); }
        if (hp & 1) B_LV(U[hp >> 1], slot + 3 * (hp >> 1)) else B_LK(U[hp >> 1], slot + 3 * (hp >> 1))
      }
      STAMP_AT(4);
    }
    _Pragma("nounroll") for (u32 spin = 0; spin < (SPIN_CAP << 4); ++spin) { if (LDS_LD(&ctl[1]) >= 4u) break; __builtin_amdgcn_s_sleep(2); }
    asm volatile("" ::: "memory");
    if (!isA) STAMP_AT(1);
    u32x4 qf[4];
    { const u32x4 qv = *(const u32x4*)(lds + L_Q + 2048 + bS * 256 + 16 * (4 * g + (n & 3)));
#pragma unroll
      for (int t = 0; t < 4; ++t) qf[t] = (n >> 2) == t ? qv : z4; }
    u32 msk[4];
#pragma unroll
    for (int r = 0; r < 4; ++r) msk[r] = (n < 8 && (n >> 1) == r) ? ((n & 1) ? 0xffff0000u : 0x0000ffffu) : 0u;
    float m = -1e30f; double l = 0.0, acc[4] = {0.0, 0.0, 0.0, 0.0};
    SB();
#define B_QK(X_) f32x4 d = {0.f, 0.f, 0.f, 0.f}; _Pragma("unroll") for (int t = 0; t < 4; ++t) d = mfma16(X_[t], qf[t], d); asm volatile("" : "+v"(d) :: "memory"); SB();
#define B_PV1(X_, t_) { u32x4 a; const u32 pb = dppu<0x150 + 4 * (t_)>(p2); \
      a[0] = pb & msk[0]; a[1] = pb & msk[1]; a[2] = pb & msk[2]; a[3] = pb & msk[3]; tv = mfma16(a, X_[4 + (t_)], tv); }
#define B_SMPV(X_, valid_) { float sc = quad_diag_sum(d) * CS; if (!(valid_)) sc = -2e30f; \
      const float fl = __builtin_floorf(sc); \
      if (__any(fl >= m)) { const float mn = wave_max(fl) + 1.f; const int e_ = idiff(m, mn); m = mn; \
        l = __builtin_ldexp(l, e_); _Pragma("unroll") for (int r = 0; r < 4; ++r) acc[r] = __builtin_ldexp(acc[r], e_); } \
      const float p = __builtin_ldexpf(ex2(sc - fl), idiff(fl, m)); l += (double)p; const u32 p2 = cvtpk(p, p); \
      f32x4 tv = {0.f, 0.f, 0.f, 0.f}; \
      B_PV1(X_, 0) B_PV1(X_, 1) B_PV1(X_, 2) B_PV1(X_, 3) asm volatile("" : "+v"(tv) :: "memory"); SB(); \
      _Pragma("unroll") for (int r = 0; r < 4; ++r) acc[r] += (double)tv[r]; }
#define B_NEXT(tn_, kk_) u32 tn_ = (u32)(slot + 3 * (kk_)); \
      if (lane == 0) __hip_atomic_fetch_max(&ctl[4], (u32)(kk_), __ATOMIC_RELAXED, __HIP_MEMORY_SCOPE_WORKGROUP); \
      if ((kk_) >= bs) { \
        _Pragma("nounroll") for (u32 spin = 0; spin < (SPIN_CAP << 4); ++spin) { \
          tn_ = __builtin_amdgcn_readfirstlane(LDS_LD(&tq[(kk_) - bs])); \
          if (tn_ != 0xffffffffu) break; \
          __builtin_amdgcn_s_sleep(1); } \
        tn_ = tn_ < 128u ? tn_ : 128u; }
#define B_TS(k_)
    bool va = true, vb = true;
    int k0 = W0T;
    if (!isA) {
      { B_QK(U[0]) B_LK(U[0], slot + 3 * W0T) B_SMPV(U[0], true) B_LV(U[0], slot + 3 * W0T) }
      { B_QK(U[1]) B_LK(U[1], slot + 3 * (W0T + 1)) B_SMPV(U[1], true) B_LV(U[1], slot + 3 * (W0T + 1)) }
#pragma unroll
      for (int pp = 2; pp < W0T; ++pp) { B_QK(U[pp]) B_SMPV(U[pp], true) }
    } else {
      B_LK(U[0], 128) B_LV(U[0], 128) B_LK(U[1], 128) B_LV(U[1], 128)
      va = false; vb = false; k0 = 0;
    }
    bool live = true;
    _Pragma("nounroll") for (int k = k0; k < bs + TQN - 2; k += 2) {
      u32 t0 = 128u, t1 = 128u;
      if (live) { B_NEXT(t0_, k + 2) t0 = t0_; }
      { B_QK(U[0]) B_LK(U[0], t0) B_SMPV(U[0], va) B_LV(U[0], t0) }
      if (va) { B_TS(k) }
      if (t0 < 128u) { B_NEXT(t1_, k + 3) t1 = t1_; }
      { B_QK(U[1]) B_LK(U[1], t1) B_SMPV(U[1], vb) B_LV(U[1], t1) }
      if (vb) { B_TS(k + 1) }
      va = t0 < 128u; vb = t1 < 128u; live = vb;
      if (!va) break;
    }
    STAMP_AT(10);
    float* pm = (float*)(lds + L_Q + 20480);
    double* pl = (double*)(lds + L_Q + 20480 + 64);
    double* pacc = (double*)(lds + L_Q + 21504);
    if (lane < 32) { *(f64x2*)&pacc[wave * 128 + 8 * n + 4 * g] = f64x2{acc[0], acc[1]}; *(f64x2*)&pacc[wave * 128 + 8 * n + 4 * g + 2] = f64x2{acc[2], acc[3]}; }
    if ((lane & 3) == 0) pl[wave * 16 + (lane >> 2)] = l;
    if (lane == 0) pm[wave] = m;
    LBAR();
    if (wave == 0) {
      const f32x4 mv = *(const f32x4*)pm;
      const float M = fmaxf(fmaxf(mv[0], mv[1]), fmaxf(mv[2], mv[3]));
      double o0 = 0.0, o1 = 0.0;
#pragma unroll
      for (int w = 0; w < 4; ++w) { const int e = idiff(mv[w], M); const f64x2 a = *(const f64x2*)&pacc[w * 128 + 2 * lane]; o0 += __builtin_ldexp(a[0], e); o1 += __builtin_ldexp(a[1], e); }
      const float h0 = (float)o0, h1 = (float)o1;
      __builtin_amdgcn_raw_buffer_store_b128(u32x4{__float_as_uint(h0), __float_as_uint(h1), cvtpk((float)(o0 - (double)h0), (float)(o1 - (double)h1)), ta},
                                             rs, WS_PB + (((h * 2 + bS) * 4 + slot) * 64 + lane) * 16, 0, 16);
    } else if (wave == 1) {
      const f32x4 mv = *(const f32x4*)pm;
      const float M = fmaxf(fmaxf(mv[0], mv[1]), fmaxf(mv[2], mv[3]));
      double L = __builtin_ldexp(pl[lane], idiff(pm[g], M));
#pragma unroll
      for (int o = 1; o < 64; o <<= 1) L += __shfl_xor(L, o);
      const float Lh = (float)L;
      if (lane < 2) gst(rs, WS_PBML + (((h * 2 + bS) * 4 + slot) * 2 + lane) * 16, __float_as_uint(lane ? (float)(L - (double)Lh) : M), __float_as_uint(lane ? 0.f : Lh), ta, tbg);
    }
    STAMP_AT(11);
  }

  {
    if (isA) {
      u32* ob = (u32*)(lds + L_Q + 16384);
      u32x4 wf[4][4];
#pragma unroll
      for (int ct = 0; ct < 4; ++ct)
#pragma unroll
        for (int ks = 0; ks < 4; ++ks) {
          const int r = 32 * ks + 8 * g + (n >> 2), sz = (n >> 2) | ((g & 1) << 2);
          const u8* rp = lds + r * 1024 + (((4 * wave + ct) ^ sz) * 32) + 8 * (lane & 3);
          const u32x2 a = tr_read(rp), bq = tr_read(rp + 4096);
          wf[ct][ks] = u32x4{a[0], a[1], bq[0], bq[1]};
        }
      if (role == 6) {
        if (wave == 4 || wave == 5) {
          const int b = wave & 1;
          const int mo = WS_PBML + ((h * 2 + b) * 8 + (lane & 7)) * 16;
          u32x4 gm = z4, ga[4];
          u32 done = 0;
          _Pragma("nounroll") for (u32 spin = 0; spin < SPIN_CAP; ++spin) {
            if (!((done >> ((lane & 7) >> 1)) & 1u)) gm = gld(rs, mo);
#pragma unroll
            for (int s = 0; s < 4; ++s) if (!((done >> s) & 1u)) ga[s] = gld(rs, WS_PB + (((h * 2 + b) * 4 + s) * 64 + lane) * 16);
            const u32 okm = (u32)__ballot(gok(gm, ta, tbg));
#pragma unroll
            for (int s = 0; s < 4; ++s) if (((okm >> (2 * s)) & 3u) == 3u && __all(ga[s][3] == ta)) done |= 1u << s;
            if (done == 15u) break;
            __builtin_amdgcn_s_sleep(TSLP);
          }
          const float ms = (n & 1) ? -3e30f : __uint_as_float(gm[0]);
          const float M = row_max(ms);
          double L = 0.0, o0 = 0.0, o1 = 0.0;
#define H1_ACC(s_) { const int e_ = idiff(dppf<0x150 + 2 * (s_)>(ms), M); \
            L += __builtin_ldexp((double)dppf<0x150 + 2 * (s_)>(__uint_as_float(gm[2])) + (double)dppf<0x151 + 2 * (s_)>(__uint_as_float(gm[0])), e_); \
            o0 += __builtin_ldexp((double)__uint_as_float(ga[s_][0]) + (double)bflo(ga[s_][2]), e_); o1 += __builtin_ldexp((double)__uint_as_float(ga[s_][1]) + (double)bfhi(ga[s_][2]), e_); }
          H1_ACC(0) H1_ACC(1) H1_ACC(2) H1_ACC(3)
          ob[b * 64 + lane] = cvtpk((float)(o0 / L), (float)(o1 / L));
        }
      } else if (wave >= 6) {
        const int pr = tid - 384, c0o = WS_CTX + ((h * 2) * 128 + pr) * 16;
        u32x4 g0 = gld(rs, c0o), g1 = gld(rs, c0o + 2048);
        GWAIT(g0, c0o, 8)
        GWAIT(g1, c0o + 2048, 8)
        ob[pr] = cvtpk(__uint_as_float(g0[0]) + __uint_as_float(g1[0]), __uint_as_float(g0[2]) + __uint_as_float(g1[2]));
      }
      LBAR();
      STAMP_AT(7);
      u32x4 of[4];
#pragma unroll
      for (int ks = 0; ks < 4; ++ks) { const u32x4 v = *(const u32x4*)&ob[(n & 1) * 64 + 16 * ks + 4 * g]; of[ks] = n < 2 ? v : z4; }
#pragma unroll
      for (int ct = 0; ct < 4; ++ct) {
        f32x4 y = {0.f, 0.f, 0.f, 0.f};
#pragma unroll
        for (int ks = 0; ks < 4; ++ks) y = mfma16(of[ks], wf[ct][ks], y);
        if (lane < 16) gst(rs, (role == 6 ? WS_YB : WS_YA) + ((64 * wave + 16 * ct + n) * 32 + h) * 16, __float_as_uint(y[0]), __float_as_uint(y[1]), ta, tbg);
      }
    }
    if ((wave & 3) == 0) {
      const int yo = (wave ? WS_YA : WS_YB) + (2 * bx * 32 + lane) * 16;
      u32x4 gy = gld(rs, yo);
      GWAIT(gy, yo, TSLP)
      const float s0 = x16_sum(row_sum(__uint_as_float(gy[0]))), s1 = x16_sum(row_sum(__uint_as_float(gy[2])));
      const float t0 = __shfl(s0, 32), t1 = __shfl(s1, 32);
      if (lane == 0) {
        u16* op = out + (wave ? 0 : 512) + 2 * bx;
        *(u32*)op = cvtpk(s0, t0); *(u32*)(op + 1024) = cvtpk(s1, t1);
      }
    }
    if (bx == 0) {
      LBAR();
      if (tid == 0) __builtin_amdgcn_raw_buffer_store_b128(u32x4{enext, e1n, 0u, 0u}, rs, WS_EPOCH, 0, 16);
    }
    STAMP_AT(8);
    STAMP_FLUSH(12);
  }
}

extern "C" void kernel_launch(void* const* d_in, const int* in_sizes, int n_in, void* d_out, int out_size, void* d_ws, size_t ws_size, hipStream_t stream) {
  constexpr int LDSB = 163840;
  (void)hipFuncSetAttribute((const void*)fused, hipFuncAttributeMaxDynamicSharedMemorySize, LDSB);
  fused<<<256, 512, LDSB, stream>>>((const u16*)d_in[0], (const u16*)d_in[1], (const u16*)d_in[2], (const u16*)d_in[3], (const u16*)d_in[4], (const u16*)d_in[5],
                                    (const u16*)d_in[6], (const u16*)d_in[7], (const u16*)d_in[8], (const u16*)d_in[9], (const u16*)d_in[10], (u16*)d_out, (unsigned char*)d_ws);
}
```

```cpp
#include <hip/hip_runtime.h>
#include <stdint.h>

typedef unsigned int u32;
typedef unsigned short u16;
typedef unsigned char u8;
typedef __attribute__((ext_vector_type(4))) unsigned u32x4;
typedef __attribute__((ext_vector_type(2))) unsigned u32x2;
typedef __attribute__((ext_vector_type(4))) float f32x4;
typedef __attribute__((ext_vector_type(2))) float f32x2;
typedef __attribute__((ext_vector_type(2))) double f64x2;
typedef __attribute__((ext_vector_type(16))) float f32x16;
typedef __attribute__((ext_vector_type(8))) __bf16 bf16x8;
typedef __attribute__((ext_vector_type(2))) __bf16 bf16x2;
typedef __attribute__((ext_vector_type(4))) short s16x4;
typedef __attribute__((address_space(3))) s16x4* lds_s16x4p;
typedef __amdgpu_buffer_rsrc_t rsrc_t;
#define DI __device__ __forceinline__

#define STAMP 0
#define BSTAT 26
#define BAHD 3
#define BW0 32
#define BPF 2
#define NOA 0
#define PFCODE 1
#define PFTLB 1
#define BAHD2 5
#define BLEFT 12
#define KAUX 2
#define VAUX 2
#define GSS 100
#define TSLP 4

constexpr int TQN = 64;
constexpr int SK = 8192, DC = 512;
constexpr float CS = 0.08838834764831845f * 1.4426950408889634f;

DI f32x4 mfma16(u32x4 a, u32x4 b, f32x4 c) {
  return __builtin_amdgcn_mfma_f32_16x16x32_bf16(__builtin_bit_cast(bf16x8, a), __builtin_bit_cast(bf16x8, b), c, 0, 0, 0);
}
DI f32x16 mfma32(u32x4 a, u32x4 b, f32x16 c) {
  return __builtin_amdgcn_mfma_f32_32x32x16_bf16(__builtin_bit_cast(bf16x8, a), __builtin_bit_cast(bf16x8, b), c, 0, 0, 0);
}
DI u32 cvtpk(float lo, float hi) { return __builtin_bit_cast(u32, __builtin_convertvector(f32x2{lo, hi}, bf16x2)); }
DI float bflo(u32 h) { return __uint_as_float(h << 16); }
DI float bfhi(u32 h) { return __uint_as_float(h & 0xffff0000u); }
template <int CTRL> DI u32 dppu(u32 x) { return (u32)__builtin_amdgcn_mov_dpp((int)x, CTRL, 0xf, 0xf, true); }
template <int CTRL> DI float dppf(float x) { return __uint_as_float(dppu<CTRL>(__float_as_uint(x))); }
DI rsrc_t mkrs(const void* p, u32 bytes) { return __builtin_amdgcn_make_buffer_rsrc((void*)p, 0, bytes, 0x00020000); }
DI u32x4 ldn(rsrc_t r, u32 voff, u32 soff) { return __builtin_amdgcn_raw_buffer_load_b128(r, voff, soff, 2); }
template <int AUX> DI u32x4 lda(rsrc_t r, u32 voff, u32 soff) { return __builtin_amdgcn_raw_buffer_load_b128(r, voff, soff, AUX); }
DI float x16_sum(float x) { auto s = __builtin_amdgcn_permlane16_swap(__float_as_uint(x), __float_as_uint(x), false, false); return __uint_as_float(s[0]) + __uint_as_float(s[1]); }
DI float x32_sum(float x) { auto t = __builtin_amdgcn_permlane32_swap(__float_as_uint(x), __float_as_uint(x), false, false); return __uint_as_float(t[0]) + __uint_as_float(t[1]); }
DI float x16_max(float x) { auto s = __builtin_amdgcn_permlane16_swap(__float_as_uint(x), __float_as_uint(x), false, false); return fmaxf(__uint_as_float(s[0]), __uint_as_float(s[1])); }
DI float x32_max(float x) { auto t = __builtin_amdgcn_permlane32_swap(__float_as_uint(x), __float_as_uint(x), false, false); return fmaxf(__uint_as_float(t[0]), __uint_as_float(t[1])); }
DI float row_max(float x) { x = fmaxf(x, dppf<0xB1>(x)); x = fmaxf(x, dppf<0x4E>(x)); x = fmaxf(x, dppf<0x141>(x)); return fmaxf(x, dppf<0x128>(x)); }
DI float row_sum(float x) { x += dppf<0xB1>(x); x += dppf<0x4E>(x); x += dppf<0x141>(x); return x + dppf<0x128>(x); }
DI float wave_max(float x) { return x32_max(x16_max(row_max(x))); }
DI float quad_diag_sum(f32x4 d) { float s = dppf<0x00>(d[0]); s += dppf<0x55>(d[1]); s += dppf<0xAA>(d[2]); s += dppf<0xFF>(d[3]); return s; }
template <int CTRL> DI double dppd(double x) { const u32x2 v = __builtin_bit_cast(u32x2, x); return __builtin_bit_cast(double, u32x2{dppu<CTRL>(v[0]), dppu<CTRL>(v[1])}); }
DI double x16_sumd(double x) { const u32x2 v = __builtin_bit_cast(u32x2, x); auto a = __builtin_amdgcn_permlane16_swap(v[0], v[0], false, false); auto b = __builtin_amdgcn_permlane16_swap(v[1], v[1], false, false);
  return __builtin_bit_cast(double, u32x2{a[0], b[0]}) + __builtin_bit_cast(double, u32x2{a[1], b[1]}); }
DI double x32_sumd(double x) { const u32x2 v = __builtin_bit_cast(u32x2, x); auto a = __builtin_amdgcn_permlane32_swap(v[0], v[0], false, false); auto b = __builtin_amdgcn_permlane32_swap(v[1], v[1], false, false);
  return __builtin_bit_cast(double, u32x2{a[0], b[0]}) + __builtin_bit_cast(double, u32x2{a[1], b[1]}); }
DI float ex2(float x) { return __builtin_amdgcn_exp2f(x); }
DI int idiff(float a, float b) { return (int)fmaxf(a - b, -4096.f); }
#define LBAR() do { asm volatile("s_waitcnt lgkmcnt(0)" ::: "memory"); __builtin_amdgcn_s_barrier(); asm volatile("" ::: "memory"); } while (0)
#define SB() __builtin_amdgcn_sched_barrier(0)
#define USE4(x_) asm volatile("" :: "v"(x_))
#define LDS_LD(p_) __hip_atomic_load(p_, __ATOMIC_RELAXED, __HIP_MEMORY_SCOPE_WORKGROUP)
#define LDS_ST(p_, v_) __hip_atomic_store(p_, v_, __ATOMIC_RELAXED, __HIP_MEMORY_SCOPE_WORKGROUP)

constexpr int WS_EPOCH = 0;
constexpr int WS_QX = 4096;
constexpr int WS_QABS = WS_QX + 32 * 2048;
constexpr int WS_PAO = WS_QABS + 2 * 32 * 2048;
constexpr int WS_PAML = WS_PAO + 32 * 64 * 1024;
constexpr int WS_PB = WS_PAML + 32 * 64 * 16;
constexpr int WS_PBML = WS_PB + 32 * 8 * 1024;
constexpr int WS_CNT = WS_PBML + 32 * 8 * 32;
constexpr int WS_CTX = WS_CNT + 64 * 128;
constexpr int WS_YA = WS_CTX + 32 * 4096;
constexpr int WS_YB = WS_YA + 512 * 512;
constexpr int WS_END = WS_YB + 512 * 512;
constexpr int WS_STAMP = 8 << 20;
constexpr u32 SPIN_CAP = 1u << 16;

DI u32x4 gld(rsrc_t rs, int off) { asm volatile("" ::: "memory"); return __builtin_amdgcn_raw_buffer_load_b128(rs, off, 0, 16); }
DI void gst(rsrc_t rs, int off, u32 v0, u32 v1, u32 ta, u32 tb) { __builtin_amdgcn_raw_buffer_store_b128(u32x4{v0, ta, v1, tb}, rs, off, 0, 16); }
DI bool gok(u32x4 g, u32 ta, u32 tb) { return g[1] == ta && g[3] == tb; }
#define GWAIT(g_, off_, slp_) { _Pragma("nounroll") for (u32 spin = 0; spin < SPIN_CAP; ++spin) { \
    const bool ok_ = gok(g_, ta, tbg); if (__all(ok_)) break; __builtin_amdgcn_s_sleep(slp_); if (!ok_) g_ = gld(rs, off_); } }

#define STAMP_AT(k_) do {} while (0)
#define STAMP_FLUSH(n_) do {} while (0)

#define TS_NOW(k_) do {} while (0)
struct Sel { u32 e[4], o[4]; };
DI Sel make_sel(int lane) {
  Sel s; const int slot = lane & 7, r0 = slot >> 1, half = slot & 1;
#pragma unroll
  for (int r = 0; r < 4; ++r) {
    s.e[r] = (r == r0) ? (half ? 0x01000c0cu : 0x0c0c0100u) : 0x0c0c0c0cu;
    s.o[r] = (r == r0) ? (half ? 0x03020c0cu : 0x0c0c0302u) : 0x0c0c0c0cu;
  }
  return s;
}
DI u32x4 place(u32 src, const u32 (&sel)[4]) {
  u32x4 a;
#pragma unroll
  for (int r = 0; r < 4; ++r) a[r] = __builtin_amdgcn_perm(0u, src, sel[r]);
  return a;
}
#define FRESH_SEL(name_) int name_##_l = lane; asm volatile("" : "+v"(name_##_l)); const Sel name_ = make_sel(name_##_l);
DI u32 off_a(u32 row, u32 ch) { return 2048u * (row >> 3) + 512u * (ch >> 2) + 64u * (row & 7) + 16u * ((ch & 3) ^ ((row >> 2) & 3)); }
DI u32x2 tr_read(const u8* p) { return __builtin_bit_cast(u32x2, __builtin_amdgcn_ds_read_tr16_b64_v4i16((lds_s16x4p)p)); }

constexpr int L_IMG = 0;
constexpr int L_SX = 65536;
constexpr int L_Q = 131072;

#define SVC_BAR(k_) { asm volatile("s_waitcnt lgkmcnt(0)" ::: "memory"); if (lane == 0) __hip_atomic_fetch_add(&ctl[0], 1u, __ATOMIC_RELAXED, __HIP_MEMORY_SCOPE_WORKGROUP); \
    _Pragma("nounroll") for (u32 spin = 0; spin < (SPIN_CAP << 4); ++spin) { if (LDS_LD(&ctl[0]) >= 4u * (k_)) break; __builtin_amdgcn_s_sleep(1); } \
    asm volatile("" ::: "memory"); }

#define CNT_BAR(w_, target_) { asm volatile("s_waitcnt lgkmcnt(0)" ::: "memory"); if (lane == 0) __hip_atomic_fetch_add(&ctl[w_], 1u, __ATOMIC_RELAXED, __HIP_MEMORY_SCOPE_WORKGROUP); \
    _Pragma("nounroll") for (u32 spin = 0; spin < (SPIN_CAP << 4); ++spin) { if (LDS_LD(&ctl[w_]) >= (target_)) break; __builtin_amdgcn_s_sleep(1); } \
    asm volatile("" ::: "memory"); }
#define CNT_WAIT(w_, target_, slp_) { _Pragma("nounroll") for (u32 spin = 0; spin < (SPIN_CAP << 4); ++spin) { if (LDS_LD(&ctl[w_]) >= (target_)) break; __builtin_amdgcn_s_sleep(slp_); } asm volatile("" ::: "memory"); }

__global__ void __launch_bounds__(512, 2) fused(const u16* __restrict__ Ax, const u16* __restrict__ Ackv, const u16* __restrict__ Awq, const u16* __restrict__ Awuk,
                                                const u16* __restrict__ Awuv, const u16* __restrict__ Awo, const u16* __restrict__ Bx, const u16* __restrict__ Kc,
                                                const u16* __restrict__ Vc, const u16* __restrict__ Bwq, const u16* __restrict__ Bwo, u16* __restrict__ out, unsigned char* ws) {
  extern __shared__ __attribute__((aligned(16))) unsigned char lds[];
  const int tid = threadIdx.x, lane = tid & 63, wave = __builtin_amdgcn_readfirstlane(tid >> 6);
  const int bx = blockIdx.x, x8 = bx & 7, jj = bx >> 3;
  const int h = jj, role = (x8 + jj) & 7;
  const int hq = 4 * x8 + (jj >> 3), rq = jj & 7;
  const int sw = wave - 4;
  const int g = lane >> 4, n = lane & 15;
  const bool isA = role >= 6;
  const rsrc_t rs = mkrs(ws, WS_END);
  STAMP_AT(0);
  const u32x2 ep = *(const __attribute__((address_space(4))) u32x2*)(ws + WS_EPOCH);
  const u32 enext = ep[0] + 1u, e1n = ep[1];
  const u32 ta = (enext * 0x9E3779B1u) ^ 0x7F4A7C15u, tbg = ((enext ^ e1n) * 0x85EBCA77u) ^ (e1n >> 7) ^ 0x2545F491u;
  const u32x4 z4 = {0u, 0u, 0u, 0u};
  u32* ctl = (u32*)(lds + L_Q);
  u32* tq = (u32*)(lds + L_Q + 8192);
  if (tid < 16) ctl[tid] = 0u;
  if (tid < TQN) tq[tid] = 0xffffffffu;
  constexpr int T0 = 3 * BSTAT;
  constexpr int W0T = BW0 / 8;
  const int bs = isA ? 2 : BSTAT;

  const int bS = (0x66 >> role) & 1, slot = role >> 1;
  const rsrc_t rk = mkrs((const u8*)Kc + ((size_t)(bS * 32 + h) << 21), 1 << 21), rv = mkrs((const u8*)Vc + ((size_t)(bS * 32 + h) << 21), 1 << 21);
  const u32 ko = (((lane >> 2) & 3) * 16 + 4 * g + (lane & 3)) * 16 + (wave & 3) * 4096, vo = lane * 16 + (wave & 3) * 4096;
#define B_LK(X_, t_) { const u32 so_ = (u32)(t_) * 16384u; _Pragma("unroll") for (int q_ = 0; q_ < 4; ++q_) X_[q_] = lda<KAUX>(rk, ko + q_ * 1024, so_); SB(); }
#define B_LV(X_, t_) { const u32 so_ = (u32)(t_) * 16384u; _Pragma("unroll") for (int q_ = 0; q_ < 4; ++q_) X_[4 + q_] = lda<VAUX>(rv, vo + q_ * 1024, so_); SB(); }

  const int aid = 2 * h + (role & 1), bA = aid >> 5, sp = aid & 31;
  const int kg = wave >> 2, cs = wave & 3, hh = lane & 31, hf = lane >> 5;
  const rsrc_t rc = mkrs(Ackv + ((size_t)bA * SK + 256 * sp + 32 * kg) * DC + 128 * cs, 4 * 65536 - 32768);
  const u32 co = g * 1024 + n * 16;
  u32x4 st[2][8];
#define A_ISSUE(s_, it_) { _Pragma("unroll") for (int i_ = 0; i_ < 8; ++i_) st[s_][i_] = ldn(rc, co + 4096 * i_, (it_) * 65536); SB(); }

  if (wave == 3) {
    unsigned long long pc; asm volatile("s_getpc_b64 %0" : "=s"(pc));
    const u32x4 cv = __builtin_amdgcn_raw_buffer_load_b128(mkrs((const void*)(pc & ~15ull), 23552), lane * 16, jj * 1024, 0);
    USE4(cv);
  }
  if (wave == 2) {
    const void* pp = lane == 0 ? (const void*)(ws + WS_PAO) : lane == 1 ? (const void*)(ws + WS_PB) : lane == 2 ? (const void*)(ws + WS_YB) : lane == 3 ? (const void*)(Awuv + h * 128) : lane == 4 ? (const void*)(Awuv + (1 << 20) + h * 128)
                   : lane == 5 ? (const void*)(Awo + h * 65536) : lane == 6 ? (const void*)(Bwo + h * 65536) : lane == 7 ? (const void*)out : (const void*)(ws + WS_QABS);
    const u32 tv = __builtin_nontemporal_load((const u32*)pp);
    asm volatile("" :: "v"(tv));
  }
  if (wave >= 4) {
    u32x4 xfB[4], xfA[4], wB[4], wA[4], wk[4];
    {
      if (rq == 0 && tid < 258) __builtin_amdgcn_raw_buffer_store_b32(0u, rs, WS_CNT + (hq * 2 + (tid & 1)) * 128, 0, 16);
      const u32 wo_ = ((128 * sw + (lane >> 1)) * 4096 + hq * 128 + 16 * rq + 8 * (lane & 1)) * 2;
      const rsrc_t r1 = mkrs(Bwq, 1 << 22), r2 = mkrs(Awq, 1 << 22), r3 = mkrs(Awuk, 1 << 22);
#pragma unroll
      for (int t = 0; t < 4; ++t) { wB[t] = ldn(r1, wo_, t * 32 * 8192); wA[t] = ldn(r2, wo_, t * 32 * 8192); }
      const int xo = (n & 1) * 512 + 128 * sw + 8 * g;
#pragma unroll
      for (int t = 0; t < 4; ++t) { xfB[t] = *(const u32x4*)(Bx + xo + 32 * t); xfA[t] = *(const u32x4*)(Ax + xo + 32 * t); }
      const u32 ko_ = ((64 * rq + 16 * sw + ((lane >> 2) & 3)) * 4096 + hq * 128 + 8 * (4 * g + (lane & 3))) * 2;
#pragma unroll
      for (int t = 0; t < 4; ++t) wk[t] = ldn(r3, ko_, t * 4 * 8192);
    }
    SB();
    LBAR();
    u8* wi = lds + L_SX + sw * 8192;
#pragma unroll
    for (int t = 0; t < 4; ++t) { *(u32x4*)(wi + t * 1024 + lane * 16) = wB[t]; *(u32x4*)(wi + 4096 + t * 1024 + lane * 16) = wA[t]; }
    const u8* rp = wi + (8 * g + (n >> 2)) * 32 + 8 * (lane & 3);
    f32x4 dB = {0.f, 0.f, 0.f, 0.f}, dA = dB;
#pragma unroll
    for (int t = 0; t < 4; ++t) {
      { const u32x2 a = tr_read(rp + t * 1024), b_ = tr_read(rp + t * 1024 + 128); dB = mfma16(n < 2 ? xfB[t] : z4, u32x4{a[0], a[1], b_[0], b_[1]}, dB); }
      { const u32x2 a = tr_read(rp + 4096 + t * 1024), b_ = tr_read(rp + 4096 + t * 1024 + 128); dA = mfma16(n < 2 ? xfA[t] : z4, u32x4{a[0], a[1], b_[0], b_[1]}, dA); }
    }
    float* qp = (float*)(lds + L_Q + 256);
    if (lane < 16) { qp[sw * 64 + n] = dB[0]; qp[sw * 64 + 16 + n] = dB[1]; qp[sw * 64 + 32 + n] = dA[0]; qp[sw * 64 + 48 + n] = dA[1]; }
    SVC_BAR(1)
    STAMP_AT(5);
    if (sw == 0 && lane < 16) {
      f32x4 s = {0.f, 0.f, 0.f, 0.f};
#pragma unroll
      for (int w_ = 0; w_ < 4; ++w_) s += *(const f32x4*)&qp[w_ * 64 + 4 * lane];
      gst(rs, WS_QX + ((hq * 8 + rq) * 16 + lane) * 16, cvtpk(s[0], s[1]), cvtpk(s[2], s[3]), ta, tbg);
    }
    if (sw >= 2) {
      const int qo = WS_QX + (((sw == 2 ? h : hq) * 8 + (lane >> 3)) * 16 + (sw - 2) * 8 + (lane & 7)) * 16;
      __builtin_amdgcn_s_sleep(8);
      u32x4 gq = gld(rs, qo);
      GWAIT(gq, qo, 2)
      *(u32x2*)(lds + L_Q + 2048 + ((sw - 2) * 2 + ((lane >> 2) & 1)) * 256 + (lane >> 3) * 32 + (lane & 3) * 8) = u32x2{gq[0], gq[2]};
    }
    SVC_BAR(2)
    STAMP_AT(6);
    const u32x4 qv = *(const u32x4*)(lds + L_Q + 2048 + 512 + ((n >> 2) & 1) * 256 + 16 * (4 * g + (n & 3)));
    const u32x4 qf0 = (n >> 3) == 0 ? qv : z4, qf1 = (n >> 3) == 1 ? qv : z4;
    float* qa = (float*)(lds + L_Q + 4096) + sw * 32;
#pragma unroll
    for (int u = 0; u < 2; ++u) {
      f32x4 d = {0.f, 0.f, 0.f, 0.f};
      d = mfma16(wk[2 * u], qf0, d); d = mfma16(wk[2 * u + 1], qf1, d);
      const float s = quad_diag_sum(d);
      if ((lane & 3) == 0) qa[((lane >> 2) & 1) * 16 + 8 * u + 4 * ((lane >> 3) & 1) + g] = s;
    }
    asm volatile("s_waitcnt lgkmcnt(0)" ::: "memory");
    if (lane < 8) { const f32x4 v = *(const f32x4*)&qa[4 * lane];
      gst(rs, WS_QABS + (((lane >> 2) * 32 + hq) * 128 + 16 * rq + 4 * sw + (lane & 3)) * 16, cvtpk(v[0], v[1]), cvtpk(v[2], v[3]), ta, tbg); }
    SB();
    if (lane == 0) __hip_atomic_fetch_add(&ctl[1], 1u, __ATOMIC_RELAXED, __HIP_MEMORY_SCOPE_WORKGROUP);
    if (isA && !NOA) { A_ISSUE(0, 0) A_ISSUE(1, 1) }
  } else {
    LBAR();
    if (isA && !NOA) { A_ISSUE(0, 0) USE4(st[0][7]); SB(); A_ISSUE(1, 1) USE4(st[1][7]); SB(); }
  }

  if (isA && !NOA) {
    u8* img = lds + L_IMG + wave * 8192;
#define A_STAGE(s_) { _Pragma("unroll") for (int i = 0; i < 8; ++i) *(u32x4*)(img + off_a(4 * i + g, n)) = st[s_][i]; asm volatile("" ::: "memory"); SB(); }
    u32x4 qh[8];
    {
      u8* qhi = lds + L_SX;
      LBAR();
      STAMP_AT(1);
      u32x4 gq[8];
#pragma unroll
      for (int k = 0; k < 8; ++k) gq[k] = gld(rs, WS_QABS + ((bA * 32 + 4 * wave) * 128 + k * 64 + lane) * 16);
      _Pragma("nounroll") for (u32 spin = 0; spin < SPIN_CAP; ++spin) {
        bool ok = true;
#pragma unroll
        for (int k = 0; k < 8; ++k) ok &= gok(gq[k], ta, tbg);
        if (__all(ok)) break;
        __builtin_amdgcn_s_sleep(8);
#pragma unroll
        for (int k = 0; k < 8; ++k) if (!gok(gq[k], ta, tbg)) gq[k] = gld(rs, WS_QABS + ((bA * 32 + 4 * wave) * 128 + k * 64 + lane) * 16);
      }
      SB();
      A_STAGE(0)
      A_ISSUE(0, 2)
#pragma unroll
      for (int k = 0; k < 8; ++k) {
        const int idx = (lane >> 1) & 15, hd = 4 * wave + (k >> 1);
        *(u32x2*)(qhi + ((2 * (k & 1) + (lane >> 5)) * 16 + idx) * 512 + ((hd ^ (idx & 7)) * 16) + (lane & 1) * 8) = u32x2{gq[k][0], gq[k][2]};
      }
      LBAR();
#pragma unroll
      for (int s = 0; s < 8; ++s) qh[s] = *(const u32x4*)(qhi + (cs * 16 + 2 * s + hf) * 512 + ((hh ^ ((2 * s + hf) & 7)) * 16));
      LBAR();
    }
    STAMP_AT(2);
    f32x16 o[4];
#pragma unroll
    for (int c = 0; c < 4; ++c)
#pragma unroll
      for (int r = 0; r < 16; ++r) o[c][r] = 0.f;
    float m = -INFINITY, l = 0.f;
    float* sx = (float*)(lds + L_SX);
    _Pragma("nounroll") for (int it2 = 0; it2 < 4; it2 += 2) {
#pragma unroll
      for (int ip = 0; ip < 2; ++ip) {
        f32x16 sa;
#pragma unroll
        for (int r = 0; r < 16; ++r) sa[r] = 0.f;
#pragma unroll
        for (int ks = 0; ks < 8; ++ks) { const u32x4 a = *(const u32x4*)(img + off_a(hh, 2 * ks + hf)); sa = mfma32(a, qh[ks], sa); }
        float* sb = sx + ip * 8192;
#pragma unroll
        for (int q = 0; q < 4; ++q) *(f32x4*)&sb[wave * 1024 + q * 256 + lane * 4] = f32x4{sa[4 * q], sa[4 * q + 1], sa[4 * q + 2], sa[4 * q + 3]};
        LBAR();
#pragma unroll
        for (int w = 0; w < 4; ++w)
#pragma unroll
          for (int q = 0; q < 4; ++q) { const f32x4 v = *(const f32x4*)&sb[(kg * 4 + w) * 1024 + q * 256 + lane * 4];
            if (w == 0) { sa[4 * q] = v[0]; sa[4 * q + 1] = v[1]; sa[4 * q + 2] = v[2]; sa[4 * q + 3] = v[3]; }
            else { sa[4 * q] += v[0]; sa[4 * q + 1] += v[1]; sa[4 * q + 2] += v[2]; sa[4 * q + 3] += v[3]; }
            if (w == 1 && q == 3) SB(); }
        float mx = sa[0];
#pragma unroll
        for (int r = 1; r < 16; ++r) mx = fmaxf(mx, sa[r]);
        mx = x32_max(mx) * CS;
        if (__any(mx > m)) {
          const float mn = fmaxf(m, mx), al = ex2(m - mn);
          m = mn; l *= al;
#pragma unroll
          for (int c = 0; c < 4; ++c) o[c] *= al;
        }
        u32x4 pf[2];
#pragma unroll
        for (int r = 0; r < 16; r += 2) {
          const float p0 = ex2(fmaf(sa[r], CS, -m)), p1 = ex2(fmaf(sa[r + 1], CS, -m));
          l += p0 + p1; pf[r >> 3][(r >> 1) & 3] = cvtpk(p0, p1);
        }
        const u32 blk = (lane >> 4) & 1, q4 = n >> 2, p4 = lane & 3;
#pragma unroll
        for (int ct = 0; ct < 4; ++ct)
#pragma unroll
          for (int ks = 0; ks < 2; ++ks) {
            const u32x2 lo = tr_read(img + off_a(16 * ks + 4 * hf + q4, 4 * ct + 2 * blk + (p4 >> 1)) + 8 * (p4 & 1));
            const u32x2 hi = tr_read(img + off_a(16 * ks + 8 + 4 * hf + q4, 4 * ct + 2 * blk + (p4 >> 1)) + 8 * (p4 & 1));
            o[ct] = mfma32(u32x4{lo[0], lo[1], hi[0], hi[1]}, pf[ks], o[ct]);
          }
        A_STAGE((ip + 1) & 1)
        A_ISSUE((ip + 1) & 1, it2 + ip + 3)
      }
    }
    STAMP_AT(3);
    {
      l = x32_sum(l);
      LBAR();
      float* ml = (float*)(lds + L_Q + 256);
      float* ob = (float*)(lds + L_IMG);
      u8* om = lds + L_SX;
      if (cs == 0 && lane < 32) *(f32x2*)&ml[(kg * 32 + hh) * 2] = f32x2{m, l};
      if (kg == 1) {
#pragma unroll
        for (int ct = 0; ct < 4; ++ct)
#pragma unroll
          for (int q = 0; q < 4; ++q) *(f32x4*)&ob[(((cs * 4 + ct) * 4 + q) * 64 + lane) * 4] = f32x4{o[ct][4 * q], o[ct][4 * q + 1], o[ct][4 * q + 2], o[ct][4 * q + 3]};
      }
      LBAR();
      if (kg == 0) {
        const f32x2 v1 = *(const f32x2*)&ml[(32 + hh) * 2];
        const float mn = fmaxf(m, v1[0]), a0 = ex2(m - mn), a1 = ex2(v1[0] - mn);
#pragma unroll
        for (int ct = 0; ct < 4; ++ct)
#pragma unroll
          for (int q = 0; q < 4; ++q) {
            const f32x4 o1 = *(const f32x4*)&ob[(((cs * 4 + ct) * 4 + q) * 64 + lane) * 4];
            f32x4 v;
#pragma unroll
            for (int e = 0; e < 4; ++e) v[e] = o[ct][4 * q + e] * a0 + o1[e] * a1;
            const int c4 = 32 * cs + 8 * ct + 2 * q + hf;
            *(u32x2*)(om + hh * 1024 + ((c4 ^ (hh & 15)) * 8)) = u32x2{cvtpk(v[0], v[1]), cvtpk(v[2], v[3])};
          }
        m = mn; l = l * a0 + v1[1] * a1;
      }
      LBAR();
#pragma unroll
      for (int i = 0; i < 4; ++i) {
        const int gi = i * 512 + tid, h2 = gi >> 6, c8 = gi & 63;
        const u32x2 v0 = *(const u32x2*)(om + h2 * 1024 + (((2 * c8) ^ (h2 & 15)) * 8)), v1 = *(const u32x2*)(om + h2 * 1024 + (((2 * c8 + 1) ^ (h2 & 15)) * 8));
        __builtin_amdgcn_raw_buffer_store_b128(u32x4{v0[0], v0[1], v1[0], v1[1]}, rs, WS_PAO + ((h2 * 2 + bA) * 32 + sp) * 1024 + c8 * 16, 0, 16);
      }
      asm volatile("s_waitcnt vmcnt(0)" ::: "memory");
      LBAR();
      if (wave == 0 && lane < 32) gst(rs, WS_PAML + ((hh * 2 + bA) * 32 + sp) * 16, __float_as_uint(m), __float_as_uint(l), ta, tbg);
    }
    STAMP_AT(4);
    SB();
    {
      const int ch = role & 1;
      u32x4 wv[8];
#pragma unroll
      for (int t = 0; t < 8; ++t) wv[t] = ldn(mkrs(Awuv, 1 << 22), ((256 * ch + 32 * wave + 8 * g) * 4096 + h * 128 + 8 * n) * 2, t * 8192);
      SB();
      const int b = wave >> 2, sq = wave & 3;
      const int mo = WS_PAML + ((h * 2 + b) * 32 + hh) * 16;
      u32x4 gm = gld(rs, mo);
      GWAIT(gm, mo, 16)
      u32x4 go[4];
#pragma unroll
      for (int s = 0; s < 4; ++s) go[s] = gld(rs, WS_PAO + ((h * 2 + b) * 32 + 8 * sq + 4 * hf + s) * 1024 + 512 * ch + hh * 16);
      const float ms = __uint_as_float(gm[0]), ls = __uint_as_float(gm[2]);
      const float M = x16_max(row_max(ms));
      float wt = ex2(ms - M);
      const float L = x16_sum(row_sum(wt * ls));
      wt = wt / L;
      f32x4 c0 = {0.f, 0.f, 0.f, 0.f}, c1 = c0;
#pragma unroll
      for (int s = 0; s < 4; ++s) { const float w_ = __shfl(wt, 8 * sq + 4 * hf + s);
        c0 += w_ * f32x4{bflo(go[s][0]), bfhi(go[s][0]), bflo(go[s][1]), bfhi(go[s][1])}; c1 += w_ * f32x4{bflo(go[s][2]), bfhi(go[s][2]), bflo(go[s][3]), bfhi(go[s][3])}; }
      float* cc = (float*)(lds + L_SX);
      *(f32x4*)&cc[((sq * 2 + hf) * 2 + b) * 256 + 8 * hh] = c0; *(f32x4*)&cc[((sq * 2 + hf) * 2 + b) * 256 + 8 * hh + 4] = c1;
      LBAR();
      FRESH_SEL(sel)
      u32 xh[4], xl[4];
#pragma unroll
      for (int k = 0; k < 2; ++k) {
        const int ci = (n >> 3) * 256 + 32 * wave + 8 * g + 4 * k;
        f32x4 v = *(const f32x4*)&cc[ci];
#pragma unroll
        for (int pp = 1; pp < 8; ++pp) v += *(const f32x4*)&cc[pp * 512 + ci];
        xh[2 * k] = cvtpk(v[0], v[1]); xh[2 * k + 1] = cvtpk(v[2], v[3]);
        xl[2 * k] = cvtpk(v[0] - bflo(xh[2 * k]), v[1] - bfhi(xh[2 * k])); xl[2 * k + 1] = cvtpk(v[2] - bflo(xh[2 * k + 1]), v[3] - bfhi(xh[2 * k + 1]));
      }
      f32x4 ca = {0.f, 0.f, 0.f, 0.f};
#pragma unroll
      for (int t = 0; t < 8; ++t) {
        ca = mfma16(place(xh[t >> 1], (t & 1) ? sel.o : sel.e), wv[t], ca);
        ca = mfma16(place(xl[t >> 1], (t & 1) ? sel.o : sel.e), wv[t], ca);
      }
      float* red = (float*)(lds + L_SX + 32768);
      *(f32x4*)&red[wave * 256 + (g >> 1) * 128 + 8 * n + 4 * (g & 1)] = ca;
      LBAR();
      if (wave >= 6) {
        const int pr = tid - 384;
        f32x2 s = {0.f, 0.f};
#pragma unroll
        for (int w = 0; w < 8; ++w) s += *(const f32x2*)&red[w * 256 + 2 * pr];
        gst(rs, WS_CTX + ((h * 2 + ch) * 128 + pr) * 16, __float_as_uint(s[0]), __float_as_uint(s[1]), ta, tbg);
      }
    }
    LBAR();
    STAMP_AT(9);
  }

  if (wave >= 4) {
    if (sw == 0) {
      const u32 coff = lane ? 0x7fffff00u : (u32)(WS_CNT + (h * 2 + bS) * 128);
      int prod = 0, left = 128 - T0;
      _Pragma("nounroll") for (u32 spin = 0; spin < (SPIN_CAP << 4); ++spin) {
        const int cns = (int)__builtin_amdgcn_readfirstlane(LDS_LD(&ctl[4]));
        const int want = cns + (left > BLEFT ? BAHD2 : BAHD) - bs;
        if (want > prod) {
          if (prod >= TQN - 8) { if (lane == 0) LDS_ST(&tq[prod], 128u); break; }
          int nc = left >= 12 * GSS ? 3 : left >= 6 * GSS ? 2 : 1;
          if (want - prod >= 2 && nc < 2) nc = 2;
          const int base = T0 + __builtin_amdgcn_readfirstlane(__builtin_amdgcn_raw_ptr_buffer_atomic_add_i32(nc, rs, coff, 0, 0));
          if (lane < nc) LDS_ST(&tq[prod + lane], (u32)(base + lane));
          prod += nc; left = 128 - base - nc;
          if (left < 0) break;
        } else __builtin_amdgcn_s_sleep(4);
      }
      if (lane == 0) LDS_ST(&ctl[5], 1u);
    }
    if (!isA) { STAMP_FLUSH(12); return; }
    u32* ob = (u32*)(lds + L_Q + 16384);
    if (sw == 1 || sw == 2) {
      const rsrc_t rw = mkrs((role == 6 ? Bwo : Awo) + (size_t)h * 65536, 131072);
      _Pragma("nounroll") for (int c = 0; c < 8; ++c) {
        u32x4 a[8];
        const int r0 = 16 * c + 8 * (sw - 1);
#pragma unroll
        for (int t = 0; t < 8; ++t) a[t] = ldn(rw, lane * 16, (r0 + t) * 1024);
#pragma unroll
        for (int t = 0; t < 8; ++t) { const int r = r0 + t; *(u32x4*)(lds + r * 1024 + (((lane >> 1) ^ ((r & 3) | ((r >> 1) & 4))) * 32) + (lane & 1) * 16) = a[t]; }
      }
      asm volatile("s_waitcnt lgkmcnt(0)" ::: "memory");
      if (lane == 0) __hip_atomic_fetch_add(&ctl[3], 1u, __ATOMIC_RELAXED, __HIP_MEMORY_SCOPE_WORKGROUP);
    } else if (sw == 3 && role == 7) {
#pragma unroll
      for (int k = 0; k < 2; ++k) {
        const int pr = 64 * k + lane, c0o = WS_CTX + ((h * 2) * 128 + pr) * 16;
        u32x4 g0 = gld(rs, c0o), g1 = gld(rs, c0o + 2048);
        GWAIT(g0, c0o, 8)
        GWAIT(g1, c0o + 2048, 8)
        ob[pr] = cvtpk(__uint_as_float(g0[0]) + __uint_as_float(g1[0]), __uint_as_float(g0[2]) + __uint_as_float(g1[2]));
      }
      asm volatile("s_waitcnt lgkmcnt(0)" ::: "memory");
      if (lane == 0) __hip_atomic_fetch_add(&ctl[6], 2u, __ATOMIC_RELAXED, __HIP_MEMORY_SCOPE_WORKGROUP);
    }
    CNT_WAIT(3, 2u, 8)
    u32x4 wf[8][4];
#pragma unroll
    for (int ct = 0; ct < 8; ++ct)
#pragma unroll
      for (int ks = 0; ks < 4; ++ks) {
        const int r = 32 * ks + 8 * g + (n >> 2), sz = (n >> 2) | ((g & 1) << 2);
        const u8* rp = lds + r * 1024 + (((8 * sw + ct) ^ sz) * 32) + 8 * (lane & 3);
        const u32x2 a = tr_read(rp), bq = tr_read(rp + 4096);
        wf[ct][ks] = u32x4{a[0], a[1], bq[0], bq[1]};
      }
    if (role == 6 && (sw == 1 || sw == 2)) {
      CNT_WAIT(5, 1u, 8)
      const int b = sw - 1;
      const int mo = WS_PBML + ((h * 2 + b) * 8 + (lane & 7)) * 16;
      u32x4 gm = z4, ga[4];
      u32 done = 0;
      _Pragma("nounroll") for (u32 spin = 0; spin < SPIN_CAP; ++spin) {
        if (!((done >> ((lane & 7) >> 1)) & 1u)) gm = gld(rs, mo);
#pragma unroll
        for (int s = 0; s < 4; ++s) if (!((done >> s) & 1u)) ga[s] = gld(rs, WS_PB + (((h * 2 + b) * 4 + s) * 64 + lane) * 16);
        const u32 okm = (u32)__ballot(gok(gm, ta, tbg));
#pragma unroll
        for (int s = 0; s < 4; ++s) if (((okm >> (2 * s)) & 3u) == 3u && __all(ga[s][3] == ta)) done |= 1u << s;
        if (done == 15u) break;
        __builtin_amdgcn_s_sleep(TSLP);
      }
      if (sw == 1) TS_NOW(12);
      const float ms = (n & 1) ? -3e30f : __uint_as_float(gm[0]);
      const float M = row_max(ms);
      double L = 0.0, o0 = 0.0, o1 = 0.0;
#define H1_ACC(s_) { const int e_ = idiff(dppf<0x150 + 2 * (s_)>(ms), M); \
        L += __builtin_ldexp((double)dppf<0x150 + 2 * (s_)>(__uint_as_float(gm[2])) + (double)dppf<0x151 + 2 * (s_)>(__uint_as_float(gm[0])), e_); \
        o0 += __builtin_ldexp((double)__uint_as_float(ga[s_][0]) + (double)bflo(ga[s_][2]), e_); o1 += __builtin_ldexp((double)__uint_as_float(ga[s_][1]) + (double)bfhi(ga[s_][2]), e_); }
      H1_ACC(0) H1_ACC(1) H1_ACC(2) H1_ACC(3)
      const float iL = 1.f / (float)L;
      ob[b * 64 + lane] = cvtpk((float)o0 * iL, (float)o1 * iL);
      asm volatile("s_waitcnt lgkmcnt(0)" ::: "memory");
      if (lane == 0) __hip_atomic_fetch_add(&ctl[6], 1u, __ATOMIC_RELAXED, __HIP_MEMORY_SCOPE_WORKGROUP);
    }
    CNT_WAIT(6, 2u, 1)
    STAMP_AT(7);
    if (sw == 1 && role == 6) TS_NOW(13);
    {
      u32x4 of[4];
#pragma unroll
      for (int ks = 0; ks < 4; ++ks) { const u32x4 v = *(const u32x4*)&ob[(n & 1) * 64 + 16 * ks + 4 * g]; of[ks] = n < 2 ? v : z4; }
      f32x4 y[8];
#pragma unroll
      for (int ct = 0; ct < 8; ++ct) y[ct] = f32x4{0.f, 0.f, 0.f, 0.f};
#pragma unroll
      for (int ks = 0; ks < 4; ++ks)
#pragma unroll
        for (int ct = 0; ct < 8; ++ct) y[ct] = mfma16(of[ks], wf[ct][ks], y[ct]);
      if (lane < 16) {
#pragma unroll
        for (int ct = 0; ct < 8; ++ct) gst(rs, (role == 6 ? WS_YB : WS_YA) + (((8 * sw + ct) * 32 + h) * 16 + n) * 16, __float_as_uint(y[ct][0]), __float_as_uint(y[ct][1]), ta, tbg);
      }
    }
    if (sw == 1 && role == 6) TS_NOW(14);
    if (role == 7) {
      CNT_WAIT(5, 1u, 8)
      float* fr = (float*)(lds + L_Q + 17408);
#pragma unroll
      for (int pt = 0; pt < 2; ++pt) {
        const int yo = (pt ? WS_YB : WS_YA) + ((h * 32 + 8 * sw) * 16 + lane) * 16;
        u32x4 g0 = gld(rs, yo), g1 = gld(rs, yo + 1024);
        _Pragma("nounroll") for (u32 spin = 0; spin < SPIN_CAP; ++spin) {
          const bool k0 = gok(g0, ta, tbg), k1 = gok(g1, ta, tbg);
          if (__all(k0 && k1)) break;
          __builtin_amdgcn_s_sleep(TSLP);
          if (!k0) g0 = gld(rs, yo);
          if (!k1) g1 = gld(rs, yo + 1024);
        }
        const float s0 = x32_sum(x16_sum(__uint_as_float(g0[0]) + __uint_as_float(g1[0]))), s1 = x32_sum(x16_sum(__uint_as_float(g0[2]) + __uint_as_float(g1[2])));
        if (lane < 16) { fr[((sw * 2 + pt) * 2) * 16 + n] = s0; fr[((sw * 2 + pt) * 2 + 1) * 16 + n] = s1; }
      }
      if (sw == 1) TS_NOW(15);
      CNT_BAR(7, 4u)
      if (sw == 0 && lane < 32) {
        f32x2 s = {0.f, 0.f};
#pragma unroll
        for (int w = 0; w < 4; ++w) s += *(const f32x2*)&fr[w * 64 + 2 * lane];
        *(u32*)&out[((lane >> 3) & 1) * 1024 + (lane >> 4) * 512 + 16 * h + 2 * (lane & 7)] = cvtpk(s[0], s[1]);
        if (h == 0 && lane == 0) __builtin_amdgcn_raw_buffer_store_b128(u32x4{enext, e1n, 0u, 0u}, rs, WS_EPOCH, 0, 16);
      }
    }
    STAMP_AT(8);
    STAMP_FLUSH(12);
  } else {
    u32x4 U[W0T][8];
    if (!isA) {
#pragma unroll
      for (int hp = 0; hp < 2 * W0T; ++hp) {
        if (hp >= BPF) { USE4(U[(hp - BPF) >> 1][((hp - BPF) & 1) * 4 + 3]); SB(); }
        if (hp & 1) B_LV(U[hp >> 1], slot + 3 * (hp >> 1)) else B_LK(U[hp >> 1], slot + 3 * (hp >> 1))
      }
      STAMP_AT(4);
    }
    _Pragma("nounroll") for (u32 spin = 0; spin < (SPIN_CAP << 4); ++spin) { if (LDS_LD(&ctl[1]) >= 4u) break; __builtin_amdgcn_s_sleep(2); }
    asm volatile("" ::: "memory");
    if (!isA) STAMP_AT(1);
    u32x4 qf[4];
    { const u32x4 qv = *(const u32x4*)(lds + L_Q + 2048 + bS * 256 + 16 * (4 * g + (n & 3)));
#pragma unroll
      for (int t = 0; t < 4; ++t) qf[t] = (n >> 2) == t ? qv : z4; }
    u32 msk[4];
#pragma unroll
    for (int r = 0; r < 4; ++r) msk[r] = (n < 8 && (n >> 1) == r) ? ((n & 1) ? 0xffff0000u : 0x0000ffffu) : 0u;
    float m = -1e30f; double l = 0.0, acc[4] = {0.0, 0.0, 0.0, 0.0};
    SB();
#define B_QK(X_) f32x4 d = {0.f, 0.f, 0.f, 0.f}; _Pragma("unroll") for (int t = 0; t < 4; ++t) d = mfma16(X_[t], qf[t], d); asm volatile("" : "+v"(d) :: "memory"); SB();
#define B_PV1(X_, t_) { u32x4 a; const u32 pb = dppu<0x150 + 4 * (t_)>(p2); \
      a[0] = pb & msk[0]; a[1] = pb & msk[1]; a[2] = pb & msk[2]; a[3] = pb & msk[3]; tv = mfma16(a, X_[4 + (t_)], tv); }
#define B_SMPV(X_, valid_) { float sc = quad_diag_sum(d) * CS; if (!(valid_)) sc = -2e30f; \
      const float fl = __builtin_floorf(sc); \
      if (__any(fl >= m)) { const float mn = wave_max(fl) + 1.f; const int e_ = idiff(m, mn); m = mn; \
        l = __builtin_ldexp(l, e_); _Pragma("unroll") for (int r = 0; r < 4; ++r) acc[r] = __builtin_ldexp(acc[r], e_); } \
      const float p = __builtin_ldexpf(ex2(sc - fl), idiff(fl, m)); l += (double)p; const u32 p2 = cvtpk(p, p); \
      f32x4 tv = {0.f, 0.f, 0.f, 0.f}; \
      B_PV1(X_, 0) B_PV1(X_, 1) B_PV1(X_, 2) B_PV1(X_, 3) asm volatile("" : "+v"(tv) :: "memory"); SB(); \
      _Pragma("unroll") for (int r = 0; r < 4; ++r) acc[r] += (double)tv[r]; }
#define B_NEXT(tn_, kk_) u32 tn_ = (u32)(slot + 3 * (kk_)); \
      if (lane == 0) __hip_atomic_fetch_max(&ctl[4], (u32)(kk_), __ATOMIC_RELAXED, __HIP_MEMORY_SCOPE_WORKGROUP); \
      if ((kk_) >= bs) { \
        _Pragma("nounroll") for (u32 spin = 0; spin < (SPIN_CAP << 4); ++spin) { \
          tn_ = __builtin_amdgcn_readfirstlane(LDS_LD(&tq[(kk_) - bs])); \
          if (tn_ != 0xffffffffu) break; \
          __builtin_amdgcn_s_sleep(1); } \
        tn_ = tn_ < 128u ? tn_ : 128u; }
#define B_TS(k_)
    bool va = true, vb = true;
    int k0 = W0T;
    if (!isA) {
      { B_QK(U[0]) B_LK(U[0], slot + 3 * W0T) B_SMPV(U[0], true) B_LV(U[0], slot + 3 * W0T) }
      { B_QK(U[1]) B_LK(U[1], slot + 3 * (W0T + 1)) B_SMPV(U[1], true) B_LV(U[1], slot + 3 * (W0T + 1)) }
#pragma unroll
      for (int pp = 2; pp < W0T; ++pp) { B_QK(U[pp]) B_SMPV(U[pp], true) }
    } else {
      B_LK(U[0], 128) B_LV(U[0], 128) B_LK(U[1], 128) B_LV(U[1], 128)
      va = false; vb = false; k0 = 0;
    }
    bool live = true;
    _Pragma("nounroll") for (int k = k0; k < bs + TQN - 2; k += 2) {
      u32 t0 = 128u, t1 = 128u;
      if (live) { B_NEXT(t0_, k + 2) t0 = t0_; }
      { B_QK(U[0]) B_LK(U[0], t0) B_SMPV(U[0], va) B_LV(U[0], t0) }
      if (va) { B_TS(k) }
      if (t0 < 128u) { B_NEXT(t1_, k + 3) t1 = t1_; }
      { B_QK(U[1]) B_LK(U[1], t1) B_SMPV(U[1], vb) B_LV(U[1], t1) }
      if (vb) { B_TS(k + 1) }
      va = t0 < 128u; vb = t1 < 128u; live = vb;
      if (!va) break;
    }
    STAMP_AT(10);
    float* pm = (float*)(lds + L_Q + 20480);
    double* pl = (double*)(lds + L_Q + 20480 + 64);
    double* pacc = (double*)(lds + L_Q + 21504);
    if (lane < 32) { *(f64x2*)&pacc[wave * 128 + 8 * n + 4 * g] = f64x2{acc[0], acc[1]}; *(f64x2*)&pacc[wave * 128 + 8 * n + 4 * g + 2] = f64x2{acc[2], acc[3]}; }
    l += dppd<0x124>(l); l += dppd<0x128>(l); l = x32_sumd(x16_sumd(l));
    if (lane == 0) { pm[wave] = m; pl[wave] = l; }
    CNT_BAR(2, 4u)
    if (wave == 0) {
      const f32x4 mv = *(const f32x4*)pm;
      const float M = fmaxf(fmaxf(mv[0], mv[1]), fmaxf(mv[2], mv[3]));
      double o0 = 0.0, o1 = 0.0;
#pragma unroll
      for (int w = 0; w < 4; ++w) { const int e = idiff(mv[w], M); const f64x2 a = *(const f64x2*)&pacc[w * 128 + 2 * lane]; o0 += __builtin_ldexp(a[0], e); o1 += __builtin_ldexp(a[1], e); }
      const float h0 = (float)o0, h1 = (float)o1;
      __builtin_amdgcn_raw_buffer_store_b128(u32x4{__float_as_uint(h0), __float_as_uint(h1), cvtpk((float)(o0 - (double)h0), (float)(o1 - (double)h1)), ta},
                                             rs, WS_PB + (((h * 2 + bS) * 4 + slot) * 64 + lane) * 16, 0, 16);
    } else if (wave == 1) {
      const f32x4 mv = *(const f32x4*)pm;
      const float M = fmaxf(fmaxf(mv[0], mv[1]), fmaxf(mv[2], mv[3]));
      double L = 0.0;
#pragma unroll
      for (int w = 0; w < 4; ++w) L += __builtin_ldexp(pl[w], idiff(mv[w], M));
      const float Lh = (float)L;
      if (lane < 2) gst(rs, WS_PBML + (((h * 2 + bS) * 4 + slot) * 2 + lane) * 16, __float_as_uint(lane ? (float)(L - (double)Lh) : M), __float_as_uint(lane ? 0.f : Lh), ta, tbg);
    }
    STAMP_AT(11);
    STAMP_FLUSH(12);
  }

}

extern "C" void kernel_launch(void* const* d_in, const int* in_sizes, int n_in, void* d_out, int out_size, void* d_ws, size_t ws_size, hipStream_t stream) {
  constexpr int LDSB = 163840;
  (void)hipFuncSetAttribute((const void*)fused, hipFuncAttributeMaxDynamicSharedMemorySize, LDSB);
  fused<<<256, 512, LDSB, stream>>>((const u16*)d_in[0], (const u16*)d_in[1], (const u16*)d_in[2], (const u16*)d_in[3], (const u16*)d_in[4], (const u16*)d_in[5],
                                    (const u16*)d_in[6], (const u16*)d_in[7], (const u16*)d_in[8], (const u16*)d_in[9], (const u16*)d_in[10], (u16*)d_out, (unsigned char*)d_ws);
}
```

```cpp
#include <hip/hip_runtime.h>
#include <stdint.h>

typedef unsigned int u32;
typedef unsigned short u16;
typedef unsigned char u8;
typedef __attribute__((ext_vector_type(4))) unsigned u32x4;
typedef __attribute__((ext_vector_type(2))) unsigned u32x2;
typedef __attribute__((ext_vector_type(4))) float f32x4;
typedef __attribute__((ext_vector_type(2))) float f32x2;
typedef __attribute__((ext_vector_type(2))) double f64x2;
typedef __attribute__((ext_vector_type(16))) float f32x16;
typedef __attribute__((ext_vector_type(8))) __bf16 bf16x8;
typedef __attribute__((ext_vector_type(2))) __bf16 bf16x2;
typedef __attribute__((ext_vector_type(4))) short s16x4;
typedef __attribute__((address_space(3))) s16x4* lds_s16x4p;
typedef __amdgpu_buffer_rsrc_t rsrc_t;
#define DI __device__ __forceinline__

#define STAMP 0
#define BSTAT 26
#define BAHD 3
#define BW0 32
#define BPF 2
#define NOA 0
#define PFCODE 1
#define PFTLB 1
#define BAHD2 5
#define BLEFT 12
#define KAUX 2
#define VAUX 2
#define NL 0
#define QREL 0
#define WAUX 0
#define FAKEW 0
#define FAKEA 0
#define PAUX 16
#define HELPMAX 1000
#define GSS 100
#define TSLP 4

constexpr int TQN = 64;
constexpr int SK = 8192, DC = 512;
constexpr float CS = 0.08838834764831845f * 1.4426950408889634f;

DI f32x4 mfma16(u32x4 a, u32x4 b, f32x4 c) {
  return __builtin_amdgcn_mfma_f32_16x16x32_bf16(__builtin_bit_cast(bf16x8, a), __builtin_bit_cast(bf16x8, b), c, 0, 0, 0);
}
DI f32x16 mfma32(u32x4 a, u32x4 b, f32x16 c) {
  return __builtin_amdgcn_mfma_f32_32x32x16_bf16(__builtin_bit_cast(bf16x8, a), __builtin_bit_cast(bf16x8, b), c, 0, 0, 0);
}
DI u32 cvtpk(float lo, float hi) { return __builtin_bit_cast(u32, __builtin_convertvector(f32x2{lo, hi}, bf16x2)); }
DI float bflo(u32 h) { return __uint_as_float(h << 16); }
DI float bfhi(u32 h) { return __uint_as_float(h & 0xffff0000u); }
template <int CTRL> DI u32 dppu(u32 x) { return (u32)__builtin_amdgcn_mov_dpp((int)x, CTRL, 0xf, 0xf, true); }
template <int CTRL> DI float dppf(float x) { return __uint_as_float(dppu<CTRL>(__float_as_uint(x))); }
DI rsrc_t mkrs(const void* p, u32 bytes) { return __builtin_amdgcn_make_buffer_rsrc((void*)p, 0, bytes, 0x00020000); }
DI u32x4 ldn(rsrc_t r, u32 voff, u32 soff) { return __builtin_amdgcn_raw_buffer_load_b128(r, voff, soff, 2); }
template <int AUX> DI u32x4 lda(rsrc_t r, u32 voff, u32 soff) { return __builtin_amdgcn_raw_buffer_load_b128(r, voff, soff, AUX); }
DI float x16_sum(float x) { auto s = __builtin_amdgcn_permlane16_swap(__float_as_uint(x), __float_as_uint(x), false, false); return __uint_as_float(s[0]) + __uint_as_float(s[1]); }
DI float x32_sum(float x) { auto t = __builtin_amdgcn_permlane32_swap(__float_as_uint(x), __float_as_uint(x), false, false); return __uint_as_float(t[0]) + __uint_as_float(t[1]); }
DI float x16_max(float x) { auto s = __builtin_amdgcn_permlane16_swap(__float_as_uint(x), __float_as_uint(x), false, false); return fmaxf(__uint_as_float(s[0]), __uint_as_float(s[1])); }
DI float x32_max(float x) { auto t = __builtin_amdgcn_permlane32_swap(__float_as_uint(x), __float_as_uint(x), false, false); return fmaxf(__uint_as_float(t[0]), __uint_as_float(t[1])); }
DI float row_max(float x) { x = fmaxf(x, dppf<0xB1>(x)); x = fmaxf(x, dppf<0x4E>(x)); x = fmaxf(x, dppf<0x141>(x)); return fmaxf(x, dppf<0x128>(x)); }
DI float row_sum(float x) { x += dppf<0xB1>(x); x += dppf<0x4E>(x); x += dppf<0x141>(x); return x + dppf<0x128>(x); }
DI float wave_max(float x) { return x32_max(x16_max(row_max(x))); }
DI float quad_diag_sum(f32x4 d) { float s = dppf<0x00>(d[0]); s += dppf<0x55>(d[1]); s += dppf<0xAA>(d[2]); s += dppf<0xFF>(d[3]); return s; }
template <int CTRL> DI double dppd(double x) { const u32x2 v = __builtin_bit_cast(u32x2, x); return __builtin_bit_cast(double, u32x2{dppu<CTRL>(v[0]), dppu<CTRL>(v[1])}); }
DI double x16_sumd(double x) { const u32x2 v = __builtin_bit_cast(u32x2, x); auto a = __builtin_amdgcn_permlane16_swap(v[0], v[0], false, false); auto b = __builtin_amdgcn_permlane16_swap(v[1], v[1], false, false);
  return __builtin_bit_cast(double, u32x2{a[0], b[0]}) + __builtin_bit_cast(double, u32x2{a[1], b[1]}); }
DI double x32_sumd(double x) { const u32x2 v = __builtin_bit_cast(u32x2, x); auto a = __builtin_amdgcn_permlane32_swap(v[0], v[0], false, false); auto b = __builtin_amdgcn_permlane32_swap(v[1], v[1], false, false);
  return __builtin_bit_cast(double, u32x2{a[0], b[0]}) + __builtin_bit_cast(double, u32x2{a[1], b[1]}); }
DI float ex2(float x) { return __builtin_amdgcn_exp2f(x); }
DI int idiff(float a, float b) { return (int)fmaxf(a - b, -4096.f); }
#define LBAR() do { asm volatile("s_waitcnt lgkmcnt(0)" ::: "memory"); __builtin_amdgcn_s_barrier(); asm volatile("" ::: "memory"); } while (0)
#define SB() __builtin_amdgcn_sched_barrier(0)
#define USE4(x_) asm volatile("" :: "v"(x_))
#define LDS_LD(p_) __hip_atomic_load(p_, __ATOMIC_RELAXED, __HIP_MEMORY_SCOPE_WORKGROUP)
#define LDS_ST(p_, v_) __hip_atomic_store(p_, v_, __ATOMIC_RELAXED, __HIP_MEMORY_SCOPE_WORKGROUP)

constexpr int WS_EPOCH = 0;
constexpr int WS_QX = 4096;
constexpr int WS_QABS = WS_QX + 32 * 2048;
constexpr int WS_PAO = WS_QABS + 2 * 32 * 2048;
constexpr int WS_PAML = WS_PAO + 32 * 64 * 1024;
constexpr int WS_PB = WS_PAML + 32 * 64 * 16;
constexpr int WS_PBML = WS_PB + 32 * 8 * 1024;
constexpr int WS_CNT = WS_PBML + 32 * 8 * 32;
constexpr int WS_CTX = WS_CNT + 64 * 128;
constexpr int WS_YA = WS_CTX + 32 * 4096;
constexpr int WS_YB = WS_YA + 512 * 512;
constexpr int WS_END = WS_YB + 512 * 512;
constexpr int WS_STAMP = 8 << 20;
constexpr u32 SPIN_CAP = 1u << 16;

DI u32x4 gld(rsrc_t rs, int off) { asm volatile("" ::: "memory"); return __builtin_amdgcn_raw_buffer_load_b128(rs, off, 0, 16); }
DI void gst(rsrc_t rs, int off, u32 v0, u32 v1, u32 ta, u32 tb) { __builtin_amdgcn_raw_buffer_store_b128(u32x4{v0, ta, v1, tb}, rs, off, 0, 16); }
DI bool gok(u32x4 g, u32 ta, u32 tb) { return g[1] == ta && g[3] == tb; }
#define GWAIT(g_, off_, slp_) { _Pragma("nounroll") for (u32 spin = 0; spin < SPIN_CAP; ++spin) { \
    const bool ok_ = gok(g_, ta, tbg); if (__all(ok_)) break; __builtin_amdgcn_s_sleep(slp_); if (!ok_) g_ = gld(rs, off_); } }

#define STAMP_AT(k_) do {} while (0)
#define STAMP_FLUSH(n_) do {} while (0)

#define TS_NOW(k_) do {} while (0)
struct Sel { u32 e[4], o[4]; };
DI Sel make_sel(int lane) {
  Sel s; const int slot = lane & 7, r0 = slot >> 1, half = slot & 1;
#pragma unroll
  for (int r = 0; r < 4; ++r) {
    s.e[r] = (r == r0) ? (half ? 0x01000c0cu : 0x0c0c0100u) : 0x0c0c0c0cu;
    s.o[r] = (r == r0) ? (half ? 0x03020c0cu : 0x0c0c0302u) : 0x0c0c0c0cu;
  }
  return s;
}
DI u32x4 place(u32 src, const u32 (&sel)[4]) {
  u32x4 a;
#pragma unroll
  for (int r = 0; r < 4; ++r) a[r] = __builtin_amdgcn_perm(0u, src, sel[r]);
  return a;
}
#define FRESH_SEL(name_) int name_##_l = lane; asm volatile("" : "+v"(name_##_l)); const Sel name_ = make_sel(name_##_l);
DI u32 off_a(u32 row, u32 ch) { return 2048u * (row >> 3) + 512u * (ch >> 2) + 64u * (row & 7) + 16u * ((ch & 3) ^ ((row >> 2) & 3)); }
DI u32x2 tr_read(const u8* p) { return __builtin_bit_cast(u32x2, __builtin_amdgcn_ds_read_tr16_b64_v4i16((lds_s16x4p)p)); }

constexpr int L_IMG = 0;
constexpr int L_SX = 65536;
constexpr int L_Q = 131072;

#define SVC_BAR(k_) { asm volatile("s_waitcnt lgkmcnt(0)" ::: "memory"); if (lane == 0) __hip_atomic_fetch_add(&ctl[0], 1u, __ATOMIC_RELAXED, __HIP_MEMORY_SCOPE_WORKGROUP); \
    _Pragma("nounroll") for (u32 spin = 0; spin < (SPIN_CAP << 4); ++spin) { if (LDS_LD(&ctl[0]) >= 4u * (k_)) break; __builtin_amdgcn_s_sleep(1); } \
    asm volatile("" ::: "memory"); }

#define CNT_BAR(w_, target_) { asm volatile("s_waitcnt lgkmcnt(0)" ::: "memory"); if (lane == 0) __hip_atomic_fetch_add(&ctl[w_], 1u, __ATOMIC_RELAXED, __HIP_MEMORY_SCOPE_WORKGROUP); \
    _Pragma("nounroll") for (u32 spin = 0; spin < (SPIN_CAP << 4); ++spin) { if (LDS_LD(&ctl[w_]) >= (target_)) break; __builtin_amdgcn_s_sleep(1); } \
    asm volatile("" ::: "memory"); }
#define CNT_WAIT(w_, target_, slp_) { _Pragma("nounroll") for (u32 spin = 0; spin < (SPIN_CAP << 4); ++spin) { if (LDS_LD(&ctl[w_]) >= (target_)) break; __builtin_amdgcn_s_sleep(slp_); } asm volatile("" ::: "memory"); }

__global__ void __launch_bounds__(512, 2) fused(const u16* __restrict__ Ax, const u16* __restrict__ Ackv, const u16* __restrict__ Awq, const u16* __restrict__ Awuk,
                                                const u16* __restrict__ Awuv, const u16* __restrict__ Awo, const u16* __restrict__ Bx, const u16* __restrict__ Kc,
                                                const u16* __restrict__ Vc, const u16* __restrict__ Bwq, const u16* __restrict__ Bwo, u16* __restrict__ out, unsigned char* ws) {
  extern __shared__ __attribute__((aligned(16))) unsigned char lds[];
  const int tid = threadIdx.x, lane = tid & 63, wave = __builtin_amdgcn_readfirstlane(tid >> 6);
  const int bx = blockIdx.x, x8 = bx & 7, jj = bx >> 3;
  const int h = jj, role = (x8 + jj) & 7;
  const int hq = 4 * x8 + (jj >> 3), rq = jj & 7;
  const int sw = wave - 4;
  const int g = lane >> 4, n = lane & 15;
  const bool isA = role >= 6;
  const rsrc_t rs = mkrs(ws, WS_END);
  STAMP_AT(0);
  const u32x2 ep = *(const __attribute__((address_space(4))) u32x2*)(ws + WS_EPOCH);
  const u32 enext = ep[0] + 1u, e1n = ep[1];
  const u32 ta = (enext * 0x9E3779B1u) ^ 0x7F4A7C15u, tbg = ((enext ^ e1n) * 0x85EBCA77u) ^ (e1n >> 7) ^ 0x2545F491u;
  const u32x4 z4 = {0u, 0u, 0u, 0u};
  u32* ctl = (u32*)(lds + L_Q);
  u32* tq = (u32*)(lds + L_Q + 8192);
  if (tid < 16) ctl[tid] = 0u;
  if (tid < TQN) tq[tid] = 0xffffffffu;
  constexpr int T0 = 3 * BSTAT;
  constexpr int W0T = BW0 / 8;
  const int bs = isA ? 2 : BSTAT;

  const int bS = (0x66 >> role) & 1, slot = role >> 1;
  const rsrc_t rk = mkrs((const u8*)Kc + ((size_t)(bS * 32 + h) << 21), 1 << 21), rv = mkrs((const u8*)Vc + ((size_t)(bS * 32 + h) << 21), 1 << 21);
  const u32 ko = (((lane >> 2) & 3) * 16 + 4 * g + (lane & 3)) * 16 + (wave & 3) * 4096, vo = lane * 16 + (wave & 3) * 4096;
#define B_LK(X_, t_) { const u32 so_ = (u32)(t_) * 16384u; _Pragma("unroll") for (int q_ = 0; q_ < 4; ++q_) X_[q_] = lda<KAUX>(rk, ko + q_ * 1024, so_); SB(); }
#define B_LV(X_, t_) { const u32 so_ = (u32)(t_) * 16384u; _Pragma("unroll") for (int q_ = 0; q_ < 4; ++q_) X_[4 + q_] = lda<VAUX>(rv, vo + q_ * 1024, so_); SB(); }

  const int aid = 2 * h + (role & 1), bA = aid >> 5, sp = aid & 31;
  const int kg = wave >> 2, cs = wave & 3, hh = lane & 31, hf = lane >> 5;
  const rsrc_t rc = mkrs(Ackv + ((size_t)bA * SK + 256 * sp + 32 * kg) * DC + 128 * cs, 4 * 65536 - 32768);
  const u32 co = g * 1024 + n * 16;
  u32x4 st[2][8];
#define A_ISSUE(s_, it_) { _Pragma("unroll") for (int i_ = 0; i_ < 8; ++i_) st[s_][i_] = ldn(rc, co + 4096 * i_, (it_) * 65536); SB(); }

  if (wave >= 4) {
    u32x4 xfB[4], xfA[4], wB[4], wA[4], wk[4];
    {
      if (rq == 0 && tid < 258) __builtin_amdgcn_raw_buffer_store_b32(0u, rs, WS_CNT + (hq * 2 + (tid & 1)) * 128, 0, 16);
      const u32 wo_ = ((128 * sw + (lane >> 1)) * 4096 + hq * 128 + 16 * rq + 8 * (lane & 1)) * 2;
      const rsrc_t r1 = mkrs(Bwq, 1 << 22), r2 = mkrs(Awq, 1 << 22), r3 = mkrs(Awuk, 1 << 22);
#pragma unroll
      for (int t = 0; t < 4; ++t) { if (FAKEW & 1) { wB[t] = ldn(r1, bx * 16384 + sw * 4096 + lane * 16, t * 1024); wA[t] = ldn(r2, bx * 16384 + sw * 4096 + lane * 16, t * 1024); } else { wB[t] = lda<WAUX>(r1, wo_, t * 32 * 8192); wA[t] = lda<WAUX>(r2, wo_, t * 32 * 8192); } }
      const int xo = (n & 1) * 512 + 128 * sw + 8 * g;
#pragma unroll
      for (int t = 0; t < 4; ++t) { xfB[t] = *(const u32x4*)(Bx + xo + 32 * t); xfA[t] = *(const u32x4*)(Ax + xo + 32 * t); }
      const u32 ko_ = ((64 * rq + 16 * sw + ((lane >> 2) & 3)) * 4096 + hq * 128 + 8 * (4 * g + (lane & 3))) * 2;
#pragma unroll
      for (int t = 0; t < 4; ++t) wk[t] = (FAKEW & 2) ? ldn(r3, bx * 16384 + sw * 4096 + lane * 16, t * 1024) : ldn(r3, ko_, t * 4 * 8192);
    }
    u32x4 pfv = z4;
    if (sw == 3) {
      unsigned long long pc; asm volatile("s_getpc_b64 %0" : "=s"(pc));
      pfv = __builtin_amdgcn_raw_buffer_load_b128(mkrs((const void*)(pc & ~15ull), 23552), lane * 16, jj * 1024, 0);
    } else if (sw == 2) {
      const void* pp = lane == 0 ? (const void*)(ws + WS_PAO) : lane == 1 ? (const void*)(ws + WS_PB) : lane == 2 ? (const void*)(ws + WS_YB) : lane == 3 ? (const void*)(Awuv + h * 128) : lane == 4 ? (const void*)(Awuv + (1 << 20) + h * 128)
                     : lane == 5 ? (const void*)(Awo + h * 65536) : lane == 6 ? (const void*)(Bwo + h * 65536) : lane == 7 ? (const void*)out : (const void*)(ws + WS_QABS);
      pfv[0] = __builtin_nontemporal_load((const u32*)pp);
    }
    SB();
    LBAR();
    u8* wi = lds + L_SX + sw * 8192;
#pragma unroll
    for (int t = 0; t < 4; ++t) { *(u32x4*)(wi + t * 1024 + lane * 16) = wB[t]; *(u32x4*)(wi + 4096 + t * 1024 + lane * 16) = wA[t]; }
    const u8* rp = wi + (8 * g + (n >> 2)) * 32 + 8 * (lane & 3);
    f32x4 dB = {0.f, 0.f, 0.f, 0.f}, dA = dB;
#pragma unroll
    for (int t = 0; t < 4; ++t) {
      { const u32x2 a = tr_read(rp + t * 1024), b_ = tr_read(rp + t * 1024 + 128); dB = mfma16(n < 2 ? xfB[t] : z4, u32x4{a[0], a[1], b_[0], b_[1]}, dB); }
      { const u32x2 a = tr_read(rp + 4096 + t * 1024), b_ = tr_read(rp + 4096 + t * 1024 + 128); dA = mfma16(n < 2 ? xfA[t] : z4, u32x4{a[0], a[1], b_[0], b_[1]}, dA); }
    }
    float* qp = (float*)(lds + L_Q + 256);
    if (lane < 16) { qp[sw * 64 + n] = dB[0]; qp[sw * 64 + 16 + n] = dB[1]; qp[sw * 64 + 32 + n] = dA[0]; qp[sw * 64 + 48 + n] = dA[1]; }
    SVC_BAR(1)
    STAMP_AT(5);
    if (sw == 0 && lane < 16) {
      f32x4 s = {0.f, 0.f, 0.f, 0.f};
#pragma unroll
      for (int w_ = 0; w_ < 4; ++w_) s += *(const f32x4*)&qp[w_ * 64 + 4 * lane];
      gst(rs, WS_QX + ((hq * 8 + rq) * 16 + lane) * 16, cvtpk(s[0], s[1]), cvtpk(s[2], s[3]), ta, tbg);
    }
    if (sw >= 2) {
      const int qo = WS_QX + (((sw == 2 ? h : hq) * 8 + (lane >> 3)) * 16 + (sw - 2) * 8 + (lane & 7)) * 16;
      __builtin_amdgcn_s_sleep(8);
      u32x4 gq = gld(rs, qo);
      GWAIT(gq, qo, 2)
      *(u32x2*)(lds + L_Q + 2048 + ((sw - 2) * 2 + ((lane >> 2) & 1)) * 256 + (lane >> 3) * 32 + (lane & 3) * 8) = u32x2{gq[0], gq[2]};
    }
    USE4(pfv);
    SVC_BAR(2)
    STAMP_AT(6);
    const u32x4 qv = *(const u32x4*)(lds + L_Q + 2048 + 512 + ((n >> 2) & 1) * 256 + 16 * (4 * g + (n & 3)));
    const u32x4 qf0 = (n >> 3) == 0 ? qv : z4, qf1 = (n >> 3) == 1 ? qv : z4;
    float* qa = (float*)(lds + L_Q + 4096) + sw * 32;
#pragma unroll
    for (int u = 0; u < 2; ++u) {
      f32x4 d = {0.f, 0.f, 0.f, 0.f};
      d = mfma16(wk[2 * u], qf0, d); d = mfma16(wk[2 * u + 1], qf1, d);
      const float s = quad_diag_sum(d);
      if ((lane & 3) == 0) qa[((lane >> 2) & 1) * 16 + 8 * u + 4 * ((lane >> 3) & 1) + g] = s;
    }
    asm volatile("s_waitcnt lgkmcnt(0)" ::: "memory");
    if (lane < 8) { const f32x4 v = *(const f32x4*)&qa[4 * lane];
      gst(rs, WS_QABS + (((lane >> 2) * 32 + hq) * 128 + 16 * rq + 4 * sw + (lane & 3)) * 16, cvtpk(v[0], v[1]), cvtpk(v[2], v[3]), ta, tbg); }
    SB();
    if (lane == 0) __hip_atomic_fetch_add(&ctl[1], 1u, __ATOMIC_RELAXED, __HIP_MEMORY_SCOPE_WORKGROUP);
    if (isA && !NOA) { A_ISSUE(0, 0) A_ISSUE(1, 1) }
  } else {
    LBAR();
    if (isA && !NOA) { A_ISSUE(0, 0) USE4(st[0][7]); SB(); A_ISSUE(1, 1) USE4(st[1][7]); SB(); }
  }

  if (isA && !NOA) {
    u8* img = lds + L_IMG + wave * 8192;
#define A_STAGE(s_) { _Pragma("unroll") for (int i = 0; i < 8; ++i) *(u32x4*)(img + off_a(4 * i + g, n)) = st[s_][i]; asm volatile("" ::: "memory"); SB(); }
    u32x4 qh[8];
    {
      u8* qhi = lds + L_SX;
      LBAR();
      STAMP_AT(1);
      u32x4 gq[8];
#pragma unroll
      for (int k = 0; k < 8; ++k) gq[k] = gld(rs, WS_QABS + ((bA * 32 + 4 * wave) * 128 + k * 64 + lane) * 16);
      _Pragma("nounroll") for (u32 spin = 0; spin < SPIN_CAP; ++spin) {
        bool ok = true;
#pragma unroll
        for (int k = 0; k < 8; ++k) ok &= gok(gq[k], ta, tbg);
        if (__all(ok)) break;
        __builtin_amdgcn_s_sleep(8);
#pragma unroll
        for (int k = 0; k < 8; ++k) if (!gok(gq[k], ta, tbg)) gq[k] = gld(rs, WS_QABS + ((bA * 32 + 4 * wave) * 128 + k * 64 + lane) * 16);
      }
      SB();
      A_STAGE(0)
      A_ISSUE(0, 2)
#pragma unroll
      for (int k = 0; k < 8; ++k) {
        const int idx = (lane >> 1) & 15, hd = 4 * wave + (k >> 1);
        *(u32x2*)(qhi + ((2 * (k & 1) + (lane >> 5)) * 16 + idx) * 512 + ((hd ^ (idx & 7)) * 16) + (lane & 1) * 8) = u32x2{gq[k][0], gq[k][2]};
      }
      LBAR();
#pragma unroll
      for (int s = 0; s < 8; ++s) qh[s] = *(const u32x4*)(qhi + (cs * 16 + 2 * s + hf) * 512 + ((hh ^ ((2 * s + hf) & 7)) * 16));
      LBAR();
    }
    STAMP_AT(2);
    f32x16 o[4];
#pragma unroll
    for (int c = 0; c < 4; ++c)
#pragma unroll
      for (int r = 0; r < 16; ++r) o[c][r] = 0.f;
    float m = -INFINITY, l = 0.f;
    float* sx = (float*)(lds + L_SX);
    _Pragma("nounroll") for (int it2 = 0; it2 < 4; it2 += 2) {
#pragma unroll
      for (int ip = 0; ip < 2; ++ip) {
        f32x16 sa;
#pragma unroll
        for (int r = 0; r < 16; ++r) sa[r] = 0.f;
#pragma unroll
        for (int ks = 0; ks < 8; ++ks) { const u32x4 a = *(const u32x4*)(img + off_a(hh, 2 * ks + hf)); sa = mfma32(a, qh[ks], sa); }
        float* sb = sx + ip * 8192;
#pragma unroll
        for (int q = 0; q < 4; ++q) *(f32x4*)&sb[wave * 1024 + q * 256 + lane * 4] = f32x4{sa[4 * q], sa[4 * q + 1], sa[4 * q + 2], sa[4 * q + 3]};
        LBAR();
#pragma unroll
        for (int w = 0; w < 4; ++w)
#pragma unroll
          for (int q = 0; q < 4; ++q) { const f32x4 v = *(const f32x4*)&sb[(kg * 4 + w) * 1024 + q * 256 + lane * 4];
            if (w == 0) { sa[4 * q] = v[0]; sa[4 * q + 1] = v[1]; sa[4 * q + 2] = v[2]; sa[4 * q + 3] = v[3]; }
            else { sa[4 * q] += v[0]; sa[4 * q + 1] += v[1]; sa[4 * q + 2] += v[2]; sa[4 * q + 3] += v[3]; }
            if (w == 1 && q == 3) SB(); }
        float mx = sa[0];
#pragma unroll
        for (int r = 1; r < 16; ++r) mx = fmaxf(mx, sa[r]);
        mx = x32_max(mx) * CS;
        if (__any(mx > m)) {
          const float mn = fmaxf(m, mx), al = ex2(m - mn);
          m = mn; l *= al;
#pragma unroll
          for (int c = 0; c < 4; ++c) o[c] *= al;
        }
        u32x4 pf[2];
#pragma unroll
        for (int r = 0; r < 16; r += 2) {
          const float p0 = ex2(fmaf(sa[r], CS, -m)), p1 = ex2(fmaf(sa[r + 1], CS, -m));
          l += p0 + p1; pf[r >> 3][(r >> 1) & 3] = cvtpk(p0, p1);
        }
        const u32 blk = (lane >> 4) & 1, q4 = n >> 2, p4 = lane & 3;
#pragma unroll
        for (int ct = 0; ct < 4; ++ct)
#pragma unroll
          for (int ks = 0; ks < 2; ++ks) {
            const u32x2 lo = tr_read(img + off_a(16 * ks + 4 * hf + q4, 4 * ct + 2 * blk + (p4 >> 1)) + 8 * (p4 & 1));
            const u32x2 hi = tr_read(img + off_a(16 * ks + 8 + 4 * hf + q4, 4 * ct + 2 * blk + (p4 >> 1)) + 8 * (p4 & 1));
            o[ct] = mfma32(u32x4{lo[0], lo[1], hi[0], hi[1]}, pf[ks], o[ct]);
          }
        A_STAGE((ip + 1) & 1)
        A_ISSUE((ip + 1) & 1, it2 + ip + 3)
      }
    }
    STAMP_AT(3);
    {
      l = x32_sum(l);
      LBAR();
      float* ml = (float*)(lds + L_Q + 256);
      float* ob = (float*)(lds + L_IMG);
      u8* om = lds + L_SX;
      if (cs == 0 && lane < 32) *(f32x2*)&ml[(kg * 32 + hh) * 2] = f32x2{m, l};
      if (kg == 1) {
#pragma unroll
        for (int ct = 0; ct < 4; ++ct)
#pragma unroll
          for (int q = 0; q < 4; ++q) *(f32x4*)&ob[(((cs * 4 + ct) * 4 + q) * 64 + lane) * 4] = f32x4{o[ct][4 * q], o[ct][4 * q + 1], o[ct][4 * q + 2], o[ct][4 * q + 3]};
      }
      LBAR();
      if (kg == 0) {
        const f32x2 v1 = *(const f32x2*)&ml[(32 + hh) * 2];
        const float mn = fmaxf(m, v1[0]), a0 = ex2(m - mn), a1 = ex2(v1[0] - mn);
#pragma unroll
        for (int ct = 0; ct < 4; ++ct)
#pragma unroll
          for (int q = 0; q < 4; ++q) {
            const f32x4 o1 = *(const f32x4*)&ob[(((cs * 4 + ct) * 4 + q) * 64 + lane) * 4];
            f32x4 v;
#pragma unroll
            for (int e = 0; e < 4; ++e) v[e] = o[ct][4 * q + e] * a0 + o1[e] * a1;
            const int c4 = 32 * cs + 8 * ct + 2 * q + hf;
            *(u32x2*)(om + hh * 1024 + ((c4 ^ (hh & 15)) * 8)) = u32x2{cvtpk(v[0], v[1]), cvtpk(v[2], v[3])};
          }
        m = mn; l = l * a0 + v1[1] * a1;
      }
      LBAR();
#pragma unroll
      for (int i = 0; i < 4; ++i) {
        const int gi = i * 512 + tid, h2 = gi >> 6, c8 = gi & 63;
        const u32x2 v0 = *(const u32x2*)(om + h2 * 1024 + (((2 * c8) ^ (h2 & 15)) * 8)), v1 = *(const u32x2*)(om + h2 * 1024 + (((2 * c8 + 1) ^ (h2 & 15)) * 8));
        __builtin_amdgcn_raw_buffer_store_b128(u32x4{v0[0], v0[1], v1[0], v1[1]}, rs, (FAKEA & 1) ? 0x7fffff00 : WS_PAO + ((h2 * 2 + bA) * 32 + sp) * 1024 + c8 * 16, 0, PAUX);
      }
      asm volatile("s_waitcnt vmcnt(0)" ::: "memory");
      LBAR();
      if (wave == 0 && lane < 32) gst(rs, WS_PAML + ((hh * 2 + bA) * 32 + sp) * 16, __float_as_uint(m), __float_as_uint(l), ta, tbg);
    }
    STAMP_AT(4);
    SB();
    {
      const int ch = role & 1;
      u32x4 wv[8];
#pragma unroll
      for (int t = 0; t < 8; ++t) wv[t] = (FAKEA & 2) ? ldn(mkrs(Awuv, 0), lane * 16, t * 1024) : (FAKEW & 4) ? ldn(mkrs(Awuv, 1 << 22), aid * 65536 + wave * 8192 + lane * 16, t * 1024) : ldn(mkrs(Awuv, 1 << 22), ((256 * ch + 32 * wave + 8 * g) * 4096 + h * 128 + 8 * n) * 2, t * 8192);
      SB();
      const int b = wave >> 2, sq = wave & 3;
      const int mo = WS_PAML + ((h * 2 + b) * 32 + hh) * 16;
      u32x4 gm = gld(rs, mo);
      GWAIT(gm, mo, 16)
      u32x4 go[4];
#pragma unroll
      for (int s = 0; s < 4; ++s) go[s] = gld(rs, (FAKEA & 4) ? 0x7fffff00 : WS_PAO + ((h * 2 + b) * 32 + 8 * sq + 4 * hf + s) * 1024 + 512 * ch + hh * 16);
      const float ms = __uint_as_float(gm[0]), ls = __uint_as_float(gm[2]);
      const float M = x16_max(row_max(ms));
      float wt = ex2(ms - M);
      const float L = x16_sum(row_sum(wt * ls));
      wt = wt / L;
      f32x4 c0 = {0.f, 0.f, 0.f, 0.f}, c1 = c0;
#pragma unroll
      for (int s = 0; s < 4; ++s) { const float w_ = __shfl(wt, 8 * sq + 4 * hf + s);
        c0 += w_ * f32x4{bflo(go[s][0]), bfhi(go[s][0]), bflo(go[s][1]), bfhi(go[s][1])}; c1 += w_ * f32x4{bflo(go[s][2]), bfhi(go[s][2]), bflo(go[s][3]), bfhi(go[s][3])}; }
      float* cc = (float*)(lds + L_SX);
      *(f32x4*)&cc[((sq * 2 + hf) * 2 + b) * 256 + 8 * hh] = c0; *(f32x4*)&cc[((sq * 2 + hf) * 2 + b) * 256 + 8 * hh + 4] = c1;
      LBAR();
      FRESH_SEL(sel)
      u32 xh[4], xl[4];
#pragma unroll
      for (int k = 0; k < 2; ++k) {
        const int ci = (n >> 3) * 256 + 32 * wave + 8 * g + 4 * k;
        f32x4 v = *(const f32x4*)&cc[ci];
#pragma unroll
        for (int pp = 1; pp < 8; ++pp) v += *(const f32x4*)&cc[pp * 512 + ci];
        xh[2 * k] = cvtpk(v[0], v[1]); xh[2 * k + 1] = cvtpk(v[2], v[3]);
        xl[2 * k] = cvtpk(v[0] - bflo(xh[2 * k]), v[1] - bfhi(xh[2 * k])); xl[2 * k + 1] = cvtpk(v[2] - bflo(xh[2 * k + 1]), v[3] - bfhi(xh[2 * k + 1]));
      }
      f32x4 ca = {0.f, 0.f, 0.f, 0.f};
#pragma unroll
      for (int t = 0; t < 8; ++t) {
        ca = mfma16(place(xh[t >> 1], (t & 1) ? sel.o : sel.e), wv[t], ca);
        ca = mfma16(place(xl[t >> 1], (t & 1) ? sel.o : sel.e), wv[t], ca);
      }
      float* red = (float*)(lds + L_SX + 32768);
      *(f32x4*)&red[wave * 256 + (g >> 1) * 128 + 8 * n + 4 * (g & 1)] = ca;
      LBAR();
      if (wave >= 6) {
        const int pr = tid - 384;
        f32x2 s = {0.f, 0.f};
#pragma unroll
        for (int w = 0; w < 8; ++w) s += *(const f32x2*)&red[w * 256 + 2 * pr];
        gst(rs, WS_CTX + ((h * 2 + ch) * 128 + pr) * 16, __float_as_uint(s[0]), __float_as_uint(s[1]), ta, tbg);
      }
    }
    LBAR();
    STAMP_AT(9);
  }

  if (wave >= 4) {
    if (sw == 0) {
      const u32 coff = lane ? 0x7fffff00u : (u32)(WS_CNT + (h * 2 + bS) * 128);
      int prod = 0, left = 128 - T0;
      _Pragma("nounroll") for (u32 spin = 0; spin < (SPIN_CAP << 4); ++spin) {
        const int cns = (int)__builtin_amdgcn_readfirstlane(LDS_LD(&ctl[4]));
        const int want = cns + (left > BLEFT ? BAHD2 : BAHD) - bs;
        if (want > prod) {
          if (prod >= TQN - 8 || (isA && prod >= HELPMAX)) { if (lane == 0) LDS_ST(&tq[prod], 128u); break; }
          int nc = left >= 12 * GSS ? 3 : left >= 6 * GSS ? 2 : 1;
          if (want - prod >= 2 && nc < 2) nc = 2;
          const int base = T0 + __builtin_amdgcn_readfirstlane(__builtin_amdgcn_raw_ptr_buffer_atomic_add_i32(nc, rs, coff, 0, 0));
          if (lane < nc) LDS_ST(&tq[prod + lane], (u32)(base + lane));
          prod += nc; left = 128 - base - nc;
          if (left < 0) break;
        } else __builtin_amdgcn_s_sleep(4);
      }
      if (lane == 0) LDS_ST(&ctl[5], 1u);
    }
    if (!isA) { STAMP_FLUSH(12); return; }
    u32* ob = (u32*)(lds + L_Q + 16384);
    if (sw == 1 || sw == 2) {
      const rsrc_t rw = mkrs((role == 6 ? Bwo : Awo) + (size_t)h * 65536, 131072);
      _Pragma("nounroll") for (int c = 0; c < 8; ++c) {
        u32x4 a[8];
        const int r0 = 16 * c + 8 * (sw - 1);
#pragma unroll
        for (int t = 0; t < 8; ++t) a[t] = ldn(rw, lane * 16, (r0 + t) * 1024);
#pragma unroll
        for (int t = 0; t < 8; ++t) { const int r = r0 + t; *(u32x4*)(lds + r * 1024 + (((lane >> 1) ^ ((r & 3) | ((r >> 1) & 4))) * 32) + (lane & 1) * 16) = a[t]; }
      }
      asm volatile("s_waitcnt lgkmcnt(0)" ::: "memory");
      if (lane == 0) __hip_atomic_fetch_add(&ctl[3], 1u, __ATOMIC_RELAXED, __HIP_MEMORY_SCOPE_WORKGROUP);
    } else if (sw == 3 && role == 7) {
#pragma unroll
      for (int k = 0; k < 2; ++k) {
        const int pr = 64 * k + lane, c0o = WS_CTX + ((h * 2) * 128 + pr) * 16;
        u32x4 g0 = gld(rs, c0o), g1 = gld(rs, c0o + 2048);
        GWAIT(g0, c0o, 8)
        GWAIT(g1, c0o + 2048, 8)
        ob[pr] = cvtpk(__uint_as_float(g0[0]) + __uint_as_float(g1[0]), __uint_as_float(g0[2]) + __uint_as_float(g1[2]));
      }
      asm volatile("s_waitcnt lgkmcnt(0)" ::: "memory");
      if (lane == 0) __hip_atomic_fetch_add(&ctl[6], 2u, __ATOMIC_RELAXED, __HIP_MEMORY_SCOPE_WORKGROUP);
    }
    CNT_WAIT(3, 2u, 8)
    u32x4 wf[8][4];
#pragma unroll
    for (int ct = 0; ct < 8; ++ct)
#pragma unroll
      for (int ks = 0; ks < 4; ++ks) {
        const int r = 32 * ks + 8 * g + (n >> 2), sz = (n >> 2) | ((g & 1) << 2);
        const u8* rp = lds + r * 1024 + (((8 * sw + ct) ^ sz) * 32) + 8 * (lane & 3);
        const u32x2 a = tr_read(rp), bq = tr_read(rp + 4096);
        wf[ct][ks] = u32x4{a[0], a[1], bq[0], bq[1]};
      }
    if (role == 6 && (sw == 1 || sw == 2)) {
      CNT_WAIT(5, 1u, 8)
      const int b = sw - 1;
      const int mo = WS_PBML + ((h * 2 + b) * 8 + (lane & 7)) * 16;
      u32x4 gm = z4, ga[4];
      u32 done = 0;
      _Pragma("nounroll") for (u32 spin = 0; spin < SPIN_CAP; ++spin) {
        if (!((done >> ((lane & 7) >> 1)) & 1u)) gm = gld(rs, mo);
#pragma unroll
        for (int s = 0; s < 4; ++s) if (!((done >> s) & 1u)) ga[s] = gld(rs, WS_PB + (((h * 2 + b) * 4 + s) * 64 + lane) * 16);
        const u32 okm = (u32)__ballot(gok(gm, ta, tbg));
#pragma unroll
        for (int s = 0; s < 4; ++s) if (((okm >> (2 * s)) & 3u) == 3u && __all(ga[s][3] == ta)) done |= 1u << s;
        if (done == 15u) break;
        __builtin_amdgcn_s_sleep(TSLP);
      }
      if (sw == 1) TS_NOW(12);
      const float ms = (n & 1) ? -3e30f : __uint_as_float(gm[0]);
      const float M = row_max(ms);
      double L = 0.0, o0 = 0.0, o1 = 0.0;
#define H1_ACC(s_) { const int e_ = idiff(dppf<0x150 + 2 * (s_)>(ms), M); \
        L += __builtin_ldexp((double)dppf<0x150 + 2 * (s_)>(__uint_as_float(gm[2])) + (double)dppf<0x151 + 2 * (s_)>(__uint_as_float(gm[0])), e_); \
        o0 += __builtin_ldexp((double)__uint_as_float(ga[s_][0]) + (double)bflo(ga[s_][2]), e_); o1 += __builtin_ldexp((double)__uint_as_float(ga[s_][1]) + (double)bfhi(ga[s_][2]), e_); }
      H1_ACC(0) H1_ACC(1) H1_ACC(2) H1_ACC(3)
      const float iL = 1.f / (float)L;
      ob[b * 64 + lane] = cvtpk((float)o0 * iL, (float)o1 * iL);
      asm volatile("s_waitcnt lgkmcnt(0)" ::: "memory");
      if (lane == 0) __hip_atomic_fetch_add(&ctl[6], 1u, __ATOMIC_RELAXED, __HIP_MEMORY_SCOPE_WORKGROUP);
    }
    CNT_WAIT(6, 2u, 1)
    STAMP_AT(7);
    if (sw == 1 && role == 6) TS_NOW(13);
    {
      u32x4 of[4];
#pragma unroll
      for (int ks = 0; ks < 4; ++ks) { const u32x4 v = *(const u32x4*)&ob[(n & 1) * 64 + 16 * ks + 4 * g]; of[ks] = n < 2 ? v : z4; }
      f32x4 y[8];
#pragma unroll
      for (int ct = 0; ct < 8; ++ct) y[ct] = f32x4{0.f, 0.f, 0.f, 0.f};
#pragma unroll
      for (int ks = 0; ks < 4; ++ks)
#pragma unroll
        for (int ct = 0; ct < 8; ++ct) y[ct] = mfma16(of[ks], wf[ct][ks], y[ct]);
      if (lane < 16) {
#pragma unroll
        for (int ct = 0; ct < 8; ++ct) gst(rs, (role == 6 ? WS_YB : WS_YA) + (((8 * sw + ct) * 32 + h) * 16 + n) * 16, __float_as_uint(y[ct][0]), __float_as_uint(y[ct][1]), ta, tbg);
      }
    }
    if (sw == 1 && role == 6) TS_NOW(14);
    if (role == 7) {
      CNT_WAIT(5, 1u, 8)
      float* fr = (float*)(lds + L_Q + 17408);
#pragma unroll
      for (int pt = 0; pt < 2; ++pt) {
        const int yo = (pt ? WS_YB : WS_YA) + ((h * 32 + 8 * sw) * 16 + lane) * 16;
        u32x4 g0 = gld(rs, yo), g1 = gld(rs, yo + 1024);
        _Pragma("nounroll") for (u32 spin = 0; spin < SPIN_CAP; ++spin) {
          const bool k0 = gok(g0, ta, tbg), k1 = gok(g1, ta, tbg);
          if (__all(k0 && k1)) break;
          __builtin_amdgcn_s_sleep(TSLP);
          if (!k0) g0 = gld(rs, yo);
          if (!k1) g1 = gld(rs, yo + 1024);
        }
        const float s0 = x32_sum(x16_sum(__uint_as_float(g0[0]) + __uint_as_float(g1[0]))), s1 = x32_sum(x16_sum(__uint_as_float(g0[2]) + __uint_as_float(g1[2])));
        if (lane < 16) { fr[((sw * 2 + pt) * 2) * 16 + n] = s0; fr[((sw * 2 + pt) * 2 + 1) * 16 + n] = s1; }
      }
      if (sw == 1) TS_NOW(15);
      CNT_BAR(7, 4u)
      if (sw == 0 && lane < 32) {
        f32x2 s = {0.f, 0.f};
#pragma unroll
        for (int w = 0; w < 4; ++w) s += *(const f32x2*)&fr[w * 64 + 2 * lane];
        *(u32*)&out[((lane >> 3) & 1) * 1024 + (lane >> 4) * 512 + 16 * h + 2 * (lane & 7)] = cvtpk(s[0], s[1]);
        if (h == 0 && lane == 0) __builtin_amdgcn_raw_buffer_store_b128(u32x4{enext, e1n, 0u, 0u}, rs, WS_EPOCH, 0, 16);
      }
    }
    STAMP_AT(8);
    STAMP_FLUSH(12);
  } else {
    u32x4 U[W0T][8];
    u8* pfl = lds + L_IMG + wave * (NL * 8192) + lane * 16;
    if (!isA) {
#pragma unroll
      for (int hp = 0; hp < 2 * (NL + W0T); ++hp) {
        if (hp >= BPF) {
          const int ap = hp - BPF;
          if ((ap >> 1) < NL) {
#pragma unroll
            for (int q = 0; q < 4; ++q) *(u32x4*)(pfl + ap * 4096 + q * 1024) = U[(ap >> 1) % W0T][(ap & 1) * 4 + q];
            asm volatile("" ::: "memory");
          } else USE4(U[((ap >> 1) - NL) % W0T][(ap & 1) * 4 + 3]);
          SB();
        }
        const int tl = hp >> 1, rs_ = tl < NL ? tl % W0T : tl - NL;
        if (hp & 1) B_LV(U[rs_], slot + 3 * tl) else B_LK(U[rs_], slot + 3 * tl)
      }
      STAMP_AT(4);
    }
    _Pragma("nounroll") for (u32 spin = 0; spin < (SPIN_CAP << 4); ++spin) { if (LDS_LD(&ctl[QREL]) >= (QREL ? 4u : 8u)) break; __builtin_amdgcn_s_sleep(2); }
    asm volatile("" ::: "memory");
    if (!isA) STAMP_AT(1);
    u32x4 qf[4];
    { const u32x4 qv = *(const u32x4*)(lds + L_Q + 2048 + bS * 256 + 16 * (4 * g + (n & 3)));
#pragma unroll
      for (int t = 0; t < 4; ++t) qf[t] = (n >> 2) == t ? qv : z4; }
    u32 msk[4];
#pragma unroll
    for (int r = 0; r < 4; ++r) msk[r] = (n < 8 && (n >> 1) == r) ? ((n & 1) ? 0xffff0000u : 0x0000ffffu) : 0u;
    float m = -1e30f; double l = 0.0, acc[4] = {0.0, 0.0, 0.0, 0.0};
    SB();
#define B_QK(X_) f32x4 d = {0.f, 0.f, 0.f, 0.f}; _Pragma("unroll") for (int t = 0; t < 4; ++t) d = mfma16(X_[t], qf[t], d); asm volatile("" : "+v"(d) :: "memory"); SB();
#define B_PV1(X_, t_) { u32x4 a; const u32 pb = dppu<0x150 + 4 * (t_)>(p2); \
      a[0] = pb & msk[0]; a[1] = pb & msk[1]; a[2] = pb & msk[2]; a[3] = pb & msk[3]; tv = mfma16(a, X_[4 + (t_)], tv); }
#define B_SMPV(X_, valid_) { float sc = quad_diag_sum(d) * CS; if (!(valid_)) sc = -2e30f; \
      const float fl = __builtin_floorf(sc); \
      if (__any(fl >= m)) { const float mn = wave_max(fl) + 1.f; const int e_ = idiff(m, mn); m = mn; \
        l = __builtin_ldexp(l, e_); _Pragma("unroll") for (int r = 0; r < 4; ++r) acc[r] = __builtin_ldexp(acc[r], e_); } \
      const float p = __builtin_ldexpf(ex2(sc - fl), idiff(fl, m)); l += (double)p; const u32 p2 = cvtpk(p, p); \
      f32x4 tv = {0.f, 0.f, 0.f, 0.f}; \
      B_PV1(X_, 0) B_PV1(X_, 1) B_PV1(X_, 2) B_PV1(X_, 3) asm volatile("" : "+v"(tv) :: "memory"); SB(); \
      _Pragma("unroll") for (int r = 0; r < 4; ++r) acc[r] += (double)tv[r]; }
#define B_NEXT(tn_, kk_) u32 tn_ = (u32)(slot + 3 * (kk_)); \
      if (lane == 0) __hip_atomic_fetch_max(&ctl[4], (u32)(kk_), __ATOMIC_RELAXED, __HIP_MEMORY_SCOPE_WORKGROUP); \
      if ((kk_) >= bs) { \
        _Pragma("nounroll") for (u32 spin = 0; spin < (SPIN_CAP << 4); ++spin) { \
          tn_ = __builtin_amdgcn_readfirstlane(LDS_LD(&tq[(kk_) - bs])); \
          if (tn_ != 0xffffffffu) break; \
          __builtin_amdgcn_s_sleep(1); } \
        tn_ = tn_ < 128u ? tn_ : 128u; }
#define B_TS(k_)
    bool va = true, vb = true;
    int k0 = W0T + NL;
    if (!isA) {
      { B_QK(U[0]) B_LK(U[0], slot + 3 * (W0T + NL)) B_SMPV(U[0], true) B_LV(U[0], slot + 3 * (W0T + NL)) }
      { B_QK(U[1]) B_LK(U[1], slot + 3 * (W0T + NL + 1)) B_SMPV(U[1], true) B_LV(U[1], slot + 3 * (W0T + NL + 1)) }
#pragma unroll
      for (int pp = 2; pp < W0T; ++pp) { B_QK(U[pp]) B_SMPV(U[pp], true) }
      _Pragma("nounroll") for (int pp = 0; pp < NL; ++pp) {
#pragma unroll
        for (int q = 0; q < 8; ++q) U[2][q] = *(const u32x4*)(pfl + pp * 8192 + q * 1024);
        B_QK(U[2]) B_SMPV(U[2], true)
      }
    } else {
      B_LK(U[0], 128) B_LV(U[0], 128) B_LK(U[1], 128) B_LV(U[1], 128)
      va = false; vb = false; k0 = 0;
    }
    bool live = true;
    _Pragma("nounroll") for (int k = k0; k < bs + TQN - 2; k += 2) {
      u32 t0 = 128u, t1 = 128u;
      if (live) { B_NEXT(t0_, k + 2) t0 = t0_; }
      { B_QK(U[0]) B_LK(U[0], t0) B_SMPV(U[0], va) B_LV(U[0], t0) }
      if (va) { B_TS(k) }
      if (t0 < 128u) { B_NEXT(t1_, k + 3) t1 = t1_; }
      { B_QK(U[1]) B_LK(U[1], t1) B_SMPV(U[1], vb) B_LV(U[1], t1) }
      if (vb) { B_TS(k + 1) }
      va = t0 < 128u; vb = t1 < 128u; live = vb;
      if (!va) break;
    }
    STAMP_AT(10);
    float* pm = (float*)(lds + L_Q + 20480);
    double* pl = (double*)(lds + L_Q + 20480 + 64);
    double* pacc = (double*)(lds + L_Q + 21504);
    if (lane < 32) { *(f64x2*)&pacc[wave * 128 + 8 * n + 4 * g] = f64x2{acc[0], acc[1]}; *(f64x2*)&pacc[wave * 128 + 8 * n + 4 * g + 2] = f64x2{acc[2], acc[3]}; }
    l += dppd<0x124>(l); l += dppd<0x128>(l); l = x32_sumd(x16_sumd(l));
    if (lane == 0) { pm[wave] = m; pl[wave] = l; }
    CNT_BAR(2, 4u)
    if (wave == 0) {
      const f32x4 mv = *(const f32x4*)pm;
      const float M = fmaxf(fmaxf(mv[0], mv[1]), fmaxf(mv[2], mv[3]));
      double o0 = 0.0, o1 = 0.0;
#pragma unroll
      for (int w = 0; w < 4; ++w) { const int e = idiff(mv[w], M); const f64x2 a = *(const f64x2*)&pacc[w * 128 + 2 * lane]; o0 += __builtin_ldexp(a[0], e); o1 += __builtin_ldexp(a[1], e); }
      const float h0 = (float)o0, h1 = (float)o1;
      __builtin_amdgcn_raw_buffer_store_b128(u32x4{__float_as_uint(h0), __float_as_uint(h1), cvtpk((float)(o0 - (double)h0), (float)(o1 - (double)h1)), ta},
                                             rs, WS_PB + (((h * 2 + bS) * 4 + slot) * 64 + lane) * 16, 0, 16);
    } else if (wave == 1) {
      const f32x4 mv = *(const f32x4*)pm;
      const float M = fmaxf(fmaxf(mv[0], mv[1]), fmaxf(mv[2], mv[3]));
      double L = 0.0;
#pragma unroll
      for (int w = 0; w < 4; ++w) L += __builtin_ldexp(pl[w], idiff(mv[w], M));
      const float Lh = (float)L;
      if (lane < 2) gst(rs, WS_PBML + (((h * 2 + bS) * 4 + slot) * 2 + lane) * 16, __float_as_uint(lane ? (float)(L - (double)Lh) : M), __float_as_uint(lane ? 0.f : Lh), ta, tbg);
    }
    STAMP_AT(11);
    STAMP_FLUSH(12);
  }

}

extern "C" void kernel_launch(void* const* d_in, const int* in_sizes, int n_in, void* d_out, int out_size, void* d_ws, size_t ws_size, hipStream_t stream) {
  constexpr int LDSB = 163840;
  (void)hipFuncSetAttribute((const void*)fused, hipFuncAttributeMaxDynamicSharedMemorySize, LDSB);
  fused<<<256, 512, LDSB, stream>>>((const u16*)d_in[0], (const u16*)d_in[1], (const u16*)d_in[2], (const u16*)d_in[3], (const u16*)d_in[4], (const u16*)d_in[5],
                                    (const u16*)d_in[6], (const u16*)d_in[7], (const u16*)d_in[8], (const u16*)d_in[9], (const u16*)d_in[10], (u16*)d_out, (unsigned char*)d_ws);
}
```

```cpp
#include <hip/hip_runtime.h>
#include <stdint.h>

typedef unsigned int u32;
typedef unsigned short u16;
typedef unsigned char u8;
typedef __attribute__((ext_vector_type(4))) unsigned u32x4;
typedef __attribute__((ext_vector_type(2))) unsigned u32x2;
typedef __attribute__((ext_vector_type(4))) float f32x4;
typedef __attribute__((ext_vector_type(2))) float f32x2;
typedef __attribute__((ext_vector_type(2))) double f64x2;
typedef __attribute__((ext_vector_type(16))) float f32x16;
typedef __attribute__((ext_vector_type(8))) __bf16 bf16x8;
typedef __attribute__((ext_vector_type(2))) __bf16 bf16x2;
typedef __attribute__((ext_vector_type(4))) short s16x4;
typedef __attribute__((address_space(3))) s16x4* lds_s16x4p;
typedef __amdgpu_buffer_rsrc_t rsrc_t;
#define DI __device__ __forceinline__

constexpr int BSTAT = 30;
constexpr int BAHD = 2, BAHD2 = 4, BLEFT = 8;
constexpr int CH4 = 24, CH2 = 10;
constexpr int W0T = 4;
constexpr int BPF = 2;
constexpr int TSLP = 4;
constexpr int CODE_PF = 25088;

constexpr int TQN = 64;
constexpr int SK = 8192, DC = 512;
constexpr float CS = 0.08838834764831845f * 1.4426950408889634f;

DI f32x4 mfma16(u32x4 a, u32x4 b, f32x4 c) {
  return __builtin_amdgcn_mfma_f32_16x16x32_bf16(__builtin_bit_cast(bf16x8, a), __builtin_bit_cast(bf16x8, b), c, 0, 0, 0);
}
DI f32x16 mfma32(u32x4 a, u32x4 b, f32x16 c) {
  return __builtin_amdgcn_mfma_f32_32x32x16_bf16(__builtin_bit_cast(bf16x8, a), __builtin_bit_cast(bf16x8, b), c, 0, 0, 0);
}
DI u32 cvtpk(float lo, float hi) { return __builtin_bit_cast(u32, __builtin_convertvector(f32x2{lo, hi}, bf16x2)); }
DI float bflo(u32 h) { return __uint_as_float(h << 16); }
DI float bfhi(u32 h) { return __uint_as_float(h & 0xffff0000u); }
template <int CTRL> DI u32 dppu(u32 x) { return (u32)__builtin_amdgcn_mov_dpp((int)x, CTRL, 0xf, 0xf, true); }
template <int CTRL> DI float dppf(float x) { return __uint_as_float(dppu<CTRL>(__float_as_uint(x))); }
DI rsrc_t mkrs(const void* p, u32 bytes) { return __builtin_amdgcn_make_buffer_rsrc((void*)p, 0, bytes, 0x00020000); }
DI u32x4 ldn(rsrc_t r, u32 voff, u32 soff) { return __builtin_amdgcn_raw_buffer_load_b128(r, voff, soff, 2); }
template <int AUX> DI u32x4 lda(rsrc_t r, u32 voff, u32 soff) { return __builtin_amdgcn_raw_buffer_load_b128(r, voff, soff, AUX); }
DI float x16_sum(float x) { auto s = __builtin_amdgcn_permlane16_swap(__float_as_uint(x), __float_as_uint(x), false, false); return __uint_as_float(s[0]) + __uint_as_float(s[1]); }
DI float x32_sum(float x) { auto t = __builtin_amdgcn_permlane32_swap(__float_as_uint(x), __float_as_uint(x), false, false); return __uint_as_float(t[0]) + __uint_as_float(t[1]); }
DI float x16_max(float x) { auto s = __builtin_amdgcn_permlane16_swap(__float_as_uint(x), __float_as_uint(x), false, false); return fmaxf(__uint_as_float(s[0]), __uint_as_float(s[1])); }
DI float x32_max(float x) { auto t = __builtin_amdgcn_permlane32_swap(__float_as_uint(x), __float_as_uint(x), false, false); return fmaxf(__uint_as_float(t[0]), __uint_as_float(t[1])); }
DI float row_max(float x) { x = fmaxf(x, dppf<0xB1>(x)); x = fmaxf(x, dppf<0x4E>(x)); x = fmaxf(x, dppf<0x141>(x)); return fmaxf(x, dppf<0x128>(x)); }
DI float row_sum(float x) { x += dppf<0xB1>(x); x += dppf<0x4E>(x); x += dppf<0x141>(x); return x + dppf<0x128>(x); }
DI float wave_max(float x) { return x32_max(x16_max(row_max(x))); }
DI float quad_diag_sum(f32x4 d) { float s = dppf<0x00>(d[0]); s += dppf<0x55>(d[1]); s += dppf<0xAA>(d[2]); s += dppf<0xFF>(d[3]); return s; }
template <int CTRL> DI double dppd(double x) { const u32x2 v = __builtin_bit_cast(u32x2, x); return __builtin_bit_cast(double, u32x2{dppu<CTRL>(v[0]), dppu<CTRL>(v[1])}); }
DI double x16_sumd(double x) { const u32x2 v = __builtin_bit_cast(u32x2, x); auto a = __builtin_amdgcn_permlane16_swap(v[0], v[0], false, false); auto b = __builtin_amdgcn_permlane16_swap(v[1], v[1], false, false);
  return __builtin_bit_cast(double, u32x2{a[0], b[0]}) + __builtin_bit_cast(double, u32x2{a[1], b[1]}); }
DI double x32_sumd(double x) { const u32x2 v = __builtin_bit_cast(u32x2, x); auto a = __builtin_amdgcn_permlane32_swap(v[0], v[0], false, false); auto b = __builtin_amdgcn_permlane32_swap(v[1], v[1], false, false);
  return __builtin_bit_cast(double, u32x2{a[0], b[0]}) + __builtin_bit_cast(double, u32x2{a[1], b[1]}); }
DI float ex2(float x) { return __builtin_amdgcn_exp2f(x); }
DI int idiff(float a, float b) { return (int)fmaxf(a - b, -4096.f); }
#define LBAR() do { asm volatile("s_waitcnt lgkmcnt(0)" ::: "memory"); __builtin_amdgcn_s_barrier(); asm volatile("" ::: "memory"); } while (0)
#define SB() __builtin_amdgcn_sched_barrier(0)
#define USE4(x_) asm volatile("" :: "v"(x_))
#define LDS_LD(p_) __hip_atomic_load(p_, __ATOMIC_RELAXED, __HIP_MEMORY_SCOPE_WORKGROUP)
#define LDS_ST(p_, v_) __hip_atomic_store(p_, v_, __ATOMIC_RELAXED, __HIP_MEMORY_SCOPE_WORKGROUP)

constexpr int WS_EPOCH = 0;
constexpr int WS_QX = 4096;
constexpr int WS_QABS = WS_QX + 32 * 2048;
constexpr int WS_PAO = WS_QABS + 2 * 32 * 2048;
constexpr int WS_PAML = WS_PAO + 32 * 64 * 1024;
constexpr int WS_PB = WS_PAML + 32 * 64 * 16;
constexpr int WS_PBML = WS_PB + 32 * 8 * 1024;
constexpr int WS_CNT = WS_PBML + 32 * 8 * 32;
constexpr int WS_CTX = WS_CNT + 64 * 128;
constexpr int WS_YA = WS_CTX + 32 * 4096;
constexpr int WS_YB = WS_YA + 512 * 512;
constexpr int WS_END = WS_YB + 512 * 512;
constexpr u32 SPIN_CAP = 1u << 16;

DI u32x4 gld(rsrc_t rs, int off) { asm volatile("" ::: "memory"); return __builtin_amdgcn_raw_buffer_load_b128(rs, off, 0, 16); }
DI void gst(rsrc_t rs, int off, u32 v0, u32 v1, u32 ta, u32 tb) { __builtin_amdgcn_raw_buffer_store_b128(u32x4{v0, ta, v1, tb}, rs, off, 0, 16); }
DI bool gok(u32x4 g, u32 ta, u32 tb) { return g[1] == ta && g[3] == tb; }
#define GWAIT(g_, off_, slp_) { _Pragma("nounroll") for (u32 spin = 0; spin < SPIN_CAP; ++spin) { \
    const bool ok_ = gok(g_, ta, tbg); if (__all(ok_)) break; __builtin_amdgcn_s_sleep(slp_); if (!ok_) g_ = gld(rs, off_); } }


struct Sel { u32 e[4], o[4]; };
DI Sel make_sel(int lane) {
  Sel s; const int slot = lane & 7, r0 = slot >> 1, half = slot & 1;
#pragma unroll
  for (int r = 0; r < 4; ++r) {
    s.e[r] = (r == r0) ? (half ? 0x01000c0cu : 0x0c0c0100u) : 0x0c0c0c0cu;
    s.o[r] = (r == r0) ? (half ? 0x03020c0cu : 0x0c0c0302u) : 0x0c0c0c0cu;
  }
  return s;
}
DI u32x4 place(u32 src, const u32 (&sel)[4]) {
  u32x4 a;
#pragma unroll
  for (int r = 0; r < 4; ++r) a[r] = __builtin_amdgcn_perm(0u, src, sel[r]);
  return a;
}
#define FRESH_SEL(name_) int name_##_l = lane; asm volatile("" : "+v"(name_##_l)); const Sel name_ = make_sel(name_##_l);
DI u32 off_a(u32 row, u32 ch) { return 2048u * (row >> 3) + 512u * (ch >> 2) + 64u * (row & 7) + 16u * ((ch & 3) ^ ((row >> 2) & 3)); }
DI u32x2 tr_read(const u8* p) { return __builtin_bit_cast(u32x2, __builtin_amdgcn_ds_read_tr16_b64_v4i16((lds_s16x4p)p)); }

constexpr int L_IMG = 0;
constexpr int L_SX = 65536;
constexpr int L_Q = 131072;

#define SVC_BAR(k_) { asm volatile("s_waitcnt lgkmcnt(0)" ::: "memory"); if (lane == 0) __hip_atomic_fetch_add(&ctl[0], 1u, __ATOMIC_RELAXED, __HIP_MEMORY_SCOPE_WORKGROUP); \
    _Pragma("nounroll") for (u32 spin = 0; spin < (SPIN_CAP << 4); ++spin) { if (LDS_LD(&ctl[0]) >= 4u * (k_)) break; __builtin_amdgcn_s_sleep(1); } \
    asm volatile("" ::: "memory"); }

#define CNT_BAR(w_, target_) { asm volatile("s_waitcnt lgkmcnt(0)" ::: "memory"); if (lane == 0) __hip_atomic_fetch_add(&ctl[w_], 1u, __ATOMIC_RELAXED, __HIP_MEMORY_SCOPE_WORKGROUP); \
    _Pragma("nounroll") for (u32 spin = 0; spin < (SPIN_CAP << 4); ++spin) { if (LDS_LD(&ctl[w_]) >= (target_)) break; __builtin_amdgcn_s_sleep(1); } \
    asm volatile("" ::: "memory"); }
#define CNT_WAIT(w_, target_, slp_) { _Pragma("nounroll") for (u32 spin = 0; spin < (SPIN_CAP << 4); ++spin) { if (LDS_LD(&ctl[w_]) >= (target_)) break; __builtin_amdgcn_s_sleep(slp_); } asm volatile("" ::: "memory"); }

__global__ void __launch_bounds__(512, 2) fused(const u16* __restrict__ Ax, const u16* __restrict__ Ackv, const u16* __restrict__ Awq, const u16* __restrict__ Awuk,
                                                const u16* __restrict__ Awuv, const u16* __restrict__ Awo, const u16* __restrict__ Bx, const u16* __restrict__ Kc,
                                                const u16* __restrict__ Vc, const u16* __restrict__ Bwq, const u16* __restrict__ Bwo, u16* __restrict__ out, unsigned char* ws) {
  extern __shared__ __attribute__((aligned(16))) unsigned char lds[];
  const int tid = threadIdx.x, lane = tid & 63, wave = __builtin_amdgcn_readfirstlane(tid >> 6);
  const int bx = blockIdx.x, x8 = bx & 7, jj = bx >> 3;
  const int h = jj, role = (x8 + jj) & 7;
  const int hq = 4 * x8 + (jj >> 3), rq = jj & 7;
  const int sw = wave - 4;
  const int g = lane >> 4, n = lane & 15;
  const bool isA = role >= 6;
  const rsrc_t rs = mkrs(ws, WS_END);
  const u32x2 ep = *(const __attribute__((address_space(4))) u32x2*)(ws + WS_EPOCH);
  u32 enext = ep[0] + 1u, e1n = ep[1], ta = 0u, tbg = 0u;
#define TAKE_TAGS() { asm volatile("" : "+s"(enext), "+s"(e1n)); ta = (enext * 0x9E3779B1u) ^ 0x7F4A7C15u; tbg = ((enext ^ e1n) * 0x85EBCA77u) ^ (e1n >> 7) ^ 0x2545F491u; }
  const u32x4 z4 = {0u, 0u, 0u, 0u};
  u32* ctl = (u32*)(lds + L_Q);
  u32* tq = (u32*)(lds + L_Q + 8192);
  if (tid < 16) ctl[tid] = 0u;
  if (tid < TQN) tq[tid] = 0xffffffffu;
  constexpr int T0 = 3 * BSTAT;
  const int bs = isA ? 1 : BSTAT;

  const int bS = (0x66 >> role) & 1, slot = role >> 1;
  const rsrc_t rk = mkrs((const u8*)Kc + ((size_t)(bS * 32 + h) << 21), 1 << 21), rv = mkrs((const u8*)Vc + ((size_t)(bS * 32 + h) << 21), 1 << 21);
  const u32 ko = (((lane >> 2) & 3) * 16 + 4 * g + (lane & 3)) * 16 + (wave & 3) * 4096, vo = lane * 16 + (wave & 3) * 4096;
#define B_LK(X_, t_) { const u32 so_ = (u32)(t_) * 16384u; _Pragma("unroll") for (int q_ = 0; q_ < 4; ++q_) X_[q_] = ldn(rk, ko + q_ * 1024, so_); SB(); }
#define B_LV(X_, t_) { const u32 so_ = (u32)(t_) * 16384u; _Pragma("unroll") for (int q_ = 0; q_ < 4; ++q_) X_[4 + q_] = ldn(rv, vo + q_ * 1024, so_); SB(); }

  const int aid = 2 * h + (role & 1), bA = aid >> 5, sp = aid & 31;
  const int kg = wave >> 2, cs = wave & 3, hh = lane & 31, hf = lane >> 5;
  const rsrc_t rc = mkrs(Ackv + ((size_t)bA * SK + 256 * sp + 32 * kg) * DC + 128 * cs, 4 * 65536 - 32768);
  const u32 co = g * 1024 + n * 16;
  u32x4 st[2][8];
#define A_ISSUE(s_, it_) { _Pragma("unroll") for (int i_ = 0; i_ < 8; ++i_) st[s_][i_] = ldn(rc, co + 4096 * i_, (it_) * 65536); SB(); }

  if (wave >= 4) {
    u32x4 xfB[4], xfA[4], wB[4], wA[4], wk[4];
    {
      if (rq == 0 && tid < 258) __builtin_amdgcn_raw_buffer_store_b32(0u, rs, WS_CNT + (hq * 2 + (tid & 1)) * 128, 0, 16);
      const u32 wo_ = ((128 * sw + (lane >> 1)) * 4096 + hq * 128 + 16 * rq + 8 * (lane & 1)) * 2;
      const rsrc_t r1 = mkrs(Bwq, 1 << 22), r2 = mkrs(Awq, 1 << 22), r3 = mkrs(Awuk, 1 << 22);
#pragma unroll
      for (int t = 0; t < 4; ++t) { wB[t] = lda<0>(r1, wo_, t * 32 * 8192); wA[t] = lda<0>(r2, wo_, t * 32 * 8192); }
      const int xo = (n & 1) * 512 + 128 * sw + 8 * g;
#pragma unroll
      for (int t = 0; t < 4; ++t) { xfB[t] = *(const u32x4*)(Bx + xo + 32 * t); xfA[t] = *(const u32x4*)(Ax + xo + 32 * t); }
      const u32 ko_ = ((64 * rq + 16 * sw + ((lane >> 2) & 3)) * 4096 + hq * 128 + 8 * (4 * g + (lane & 3))) * 2;
#pragma unroll
      for (int t = 0; t < 4; ++t) wk[t] = ldn(r3, ko_, t * 4 * 8192);
    }
    u32x4 pfv = z4; u32 pfw = 0u;
    if (sw == 3) {
      unsigned long long pc; asm volatile("s_getpc_b64 %0" : "=s"(pc));
      pfv = __builtin_amdgcn_raw_buffer_load_b128(mkrs((const void*)(pc & ~15ull), CODE_PF), lane * 16, jj * 1024, 0);
    } else if (sw == 2) {
      const void* pp = lane == 0 ? (const void*)(Awuv + h * 128) : lane == 1 ? (const void*)(Awuv + (1 << 20) + h * 128) : lane == 2 ? (const void*)(Awo + h * 65536) : (const void*)(Bwo + h * 65536);
      pfv = gld(rs, (lane == 0 ? WS_PAO : lane == 1 ? WS_PB : lane == 2 ? WS_YB : WS_QABS) + bx * 128);
      pfw = __builtin_nontemporal_load((const u32*)pp);
    }
    SB();
    TAKE_TAGS()
    LBAR();
    u8* wi = lds + L_SX + sw * 8192;
#pragma unroll
    for (int t = 0; t < 4; ++t) { *(u32x4*)(wi + t * 1024 + lane * 16) = wB[t]; *(u32x4*)(wi + 4096 + t * 1024 + lane * 16) = wA[t]; }
    const u8* rp = wi + (8 * g + (n >> 2)) * 32 + 8 * (lane & 3);
    f32x4 dB = {0.f, 0.f, 0.f, 0.f}, dA = dB;
#pragma unroll
    for (int t = 0; t < 4; ++t) {
      { const u32x2 a = tr_read(rp + t * 1024), b_ = tr_read(rp + t * 1024 + 128); dB = mfma16(n < 2 ? xfB[t] : z4, u32x4{a[0], a[1], b_[0], b_[1]}, dB); }
      { const u32x2 a = tr_read(rp + 4096 + t * 1024), b_ = tr_read(rp + 4096 + t * 1024 + 128); dA = mfma16(n < 2 ? xfA[t] : z4, u32x4{a[0], a[1], b_[0], b_[1]}, dA); }
    }
    float* qp = (float*)(lds + L_Q + 256);
    if (lane < 16) { qp[sw * 64 + n] = dB[0]; qp[sw * 64 + 16 + n] = dB[1]; qp[sw * 64 + 32 + n] = dA[0]; qp[sw * 64 + 48 + n] = dA[1]; }
    SVC_BAR(1)
    if (sw == 0 && lane < 16) {
      f32x4 s = {0.f, 0.f, 0.f, 0.f};
#pragma unroll
      for (int w_ = 0; w_ < 4; ++w_) s += *(const f32x4*)&qp[w_ * 64 + 4 * lane];
      if (rq == 0) asm volatile("s_waitcnt vmcnt(0)" ::: "memory");
      gst(rs, WS_QX + ((hq * 8 + rq) * 16 + lane) * 16, cvtpk(s[0], s[1]), cvtpk(s[2], s[3]), ta, tbg);
    }
    if (sw >= 2) {
      const int qo = WS_QX + (((sw == 2 ? h : hq) * 8 + (lane >> 3)) * 16 + (sw - 2) * 8 + (lane & 7)) * 16;
      __builtin_amdgcn_s_sleep(8);
      u32x4 gq = gld(rs, qo);
      GWAIT(gq, qo, 2)
      *(u32x2*)(lds + L_Q + 2048 + ((sw - 2) * 2 + ((lane >> 2) & 1)) * 256 + (lane >> 3) * 32 + (lane & 3) * 8) = u32x2{gq[0], gq[2]};
    }
    USE4(pfv); USE4(pfw);
    SVC_BAR(2)
    const u32x4 qv = *(const u32x4*)(lds + L_Q + 2048 + 512 + ((n >> 2) & 1) * 256 + 16 * (4 * g + (n & 3)));
    const u32x4 qf0 = (n >> 3) == 0 ? qv : z4, qf1 = (n >> 3) == 1 ? qv : z4;
    float* qa = (float*)(lds + L_Q + 4096) + sw * 32;
#pragma unroll
    for (int u = 0; u < 2; ++u) {
      f32x4 d = {0.f, 0.f, 0.f, 0.f};
      d = mfma16(wk[2 * u], qf0, d); d = mfma16(wk[2 * u + 1], qf1, d);
      const float s = quad_diag_sum(d);
      if ((lane & 3) == 0) qa[((lane >> 2) & 1) * 16 + 8 * u + 4 * ((lane >> 3) & 1) + g] = s;
    }
    asm volatile("s_waitcnt lgkmcnt(0)" ::: "memory");
    if (lane < 8) { const f32x4 v = *(const f32x4*)&qa[4 * lane];
      gst(rs, WS_QABS + (((lane >> 2) * 32 + hq) * 128 + 16 * rq + 4 * sw + (lane & 3)) * 16, cvtpk(v[0], v[1]), cvtpk(v[2], v[3]), ta, tbg); }
    SB();
    if (isA) { A_ISSUE(0, 0) A_ISSUE(1, 1) }
  } else {
    TAKE_TAGS()
    if (isA) LBAR();
    if (isA) { A_ISSUE(0, 0) USE4(st[0][7]); SB(); A_ISSUE(1, 1) USE4(st[1][7]); SB(); }
  }

  if (isA) {
    u8* img = lds + L_IMG + wave * 8192;
#define A_STAGE(s_) { _Pragma("unroll") for (int i = 0; i < 8; ++i) *(u32x4*)(img + off_a(4 * i + g, n)) = st[s_][i]; asm volatile("" ::: "memory"); SB(); }
    u32x4 qh[8];
    {
      u8* qhi = lds + L_SX;
      LBAR();
      A_STAGE(0)
      A_ISSUE(0, 2)
      u32x4 gq[8];
#pragma unroll
      for (int k = 0; k < 8; ++k) gq[k] = gld(rs, WS_QABS + ((bA * 32 + 4 * wave) * 128 + k * 64 + lane) * 16);
      _Pragma("nounroll") for (u32 spin = 0; spin < SPIN_CAP; ++spin) {
        bool ok = true;
#pragma unroll
        for (int k = 0; k < 8; ++k) ok &= gok(gq[k], ta, tbg);
        if (__all(ok)) break;
        __builtin_amdgcn_s_sleep(8);
#pragma unroll
        for (int k = 0; k < 8; ++k) if (!gok(gq[k], ta, tbg)) gq[k] = gld(rs, WS_QABS + ((bA * 32 + 4 * wave) * 128 + k * 64 + lane) * 16);
      }
      SB();
#pragma unroll
      for (int k = 0; k < 8; ++k) {
        const int idx = (lane >> 1) & 15, hd = 4 * wave + (k >> 1);
        *(u32x2*)(qhi + ((2 * (k & 1) + (lane >> 5)) * 16 + idx) * 512 + ((hd ^ (idx & 7)) * 16) + (lane & 1) * 8) = u32x2{gq[k][0], gq[k][2]};
      }
      LBAR();
#pragma unroll
      for (int s = 0; s < 8; ++s) qh[s] = *(const u32x4*)(qhi + (cs * 16 + 2 * s + hf) * 512 + ((hh ^ ((2 * s + hf) & 7)) * 16));
      LBAR();
    }
    f32x16 o[4];
#pragma unroll
    for (int c = 0; c < 4; ++c)
#pragma unroll
      for (int r = 0; r < 16; ++r) o[c][r] = 0.f;
    float m = -INFINITY, l = 0.f;
    float* sx = (float*)(lds + L_SX);
    _Pragma("nounroll") for (int it2 = 0; it2 < 4; it2 += 2) {
#pragma unroll
      for (int ip = 0; ip < 2; ++ip) {
        f32x16 sa;
#pragma unroll
        for (int r = 0; r < 16; ++r) sa[r] = 0.f;
#pragma unroll
        for (int ks = 0; ks < 8; ++ks) { const u32x4 a = *(const u32x4*)(img + off_a(hh, 2 * ks + hf)); sa = mfma32(a, qh[ks], sa); }
        float* sb = sx + ip * 8192;
#pragma unroll
        for (int q = 0; q < 4; ++q) *(f32x4*)&sb[wave * 1024 + q * 256 + lane * 4] = f32x4{sa[4 * q], sa[4 * q + 1], sa[4 * q + 2], sa[4 * q + 3]};
        LBAR();
#pragma unroll
        for (int w = 0; w < 4; ++w)
#pragma unroll
          for (int q = 0; q < 4; ++q) { const f32x4 v = *(const f32x4*)&sb[(kg * 4 + w) * 1024 + q * 256 + lane * 4];
            if (w == 0) { sa[4 * q] = v[0]; sa[4 * q + 1] = v[1]; sa[4 * q + 2] = v[2]; sa[4 * q + 3] = v[3]; }
            else { sa[4 * q] += v[0]; sa[4 * q + 1] += v[1]; sa[4 * q + 2] += v[2]; sa[4 * q + 3] += v[3]; }
            if (w == 1 && q == 3) SB(); }
        float mx = sa[0];
#pragma unroll
        for (int r = 1; r < 16; ++r) mx = fmaxf(mx, sa[r]);
        mx = x32_max(mx) * CS;
        if (__any(mx > m)) {
          const float mn = fmaxf(m, mx), al = ex2(m - mn);
          m = mn; l *= al;
#pragma unroll
          for (int c = 0; c < 4; ++c) o[c] *= al;
        }
        u32x4 pf[2];
#pragma unroll
        for (int r = 0; r < 16; r += 2) {
          const float p0 = ex2(fmaf(sa[r], CS, -m)), p1 = ex2(fmaf(sa[r + 1], CS, -m));
          l += p0 + p1; pf[r >> 3][(r >> 1) & 3] = cvtpk(p0, p1);
        }
        const u32 blk = (lane >> 4) & 1, q4 = n >> 2, p4 = lane & 3;
#pragma unroll
        for (int ct = 0; ct < 4; ++ct)
#pragma unroll
          for (int ks = 0; ks < 2; ++ks) {
            const u32x2 lo = tr_read(img + off_a(16 * ks + 4 * hf + q4, 4 * ct + 2 * blk + (p4 >> 1)) + 8 * (p4 & 1));
            const u32x2 hi = tr_read(img + off_a(16 * ks + 8 + 4 * hf + q4, 4 * ct + 2 * blk + (p4 >> 1)) + 8 * (p4 & 1));
            o[ct] = mfma32(u32x4{lo[0], lo[1], hi[0], hi[1]}, pf[ks], o[ct]);
          }
        A_STAGE((ip + 1) & 1)
        A_ISSUE((ip + 1) & 1, it2 + ip + 3)
      }
    }
    {
      l = x32_sum(l);
      LBAR();
      float* ml = (float*)(lds + L_Q + 256);
      float* ob = (float*)(lds + L_IMG);
      u8* om = lds + L_SX;
      if (cs == 0 && lane < 32) *(f32x2*)&ml[(kg * 32 + hh) * 2] = f32x2{m, l};
      if (kg == 1) {
#pragma unroll
        for (int ct = 0; ct < 4; ++ct)
#pragma unroll
          for (int q = 0; q < 4; ++q) *(f32x4*)&ob[(((cs * 4 + ct) * 4 + q) * 64 + lane) * 4] = f32x4{o[ct][4 * q], o[ct][4 * q + 1], o[ct][4 * q + 2], o[ct][4 * q + 3]};
      }
      LBAR();
      if (kg == 0) {
        const f32x2 v1 = *(const f32x2*)&ml[(32 + hh) * 2];
        const float mn = fmaxf(m, v1[0]), a0 = ex2(m - mn), a1 = ex2(v1[0] - mn);
#pragma unroll
        for (int ct = 0; ct < 4; ++ct)
#pragma unroll
          for (int q = 0; q < 4; ++q) {
            const f32x4 o1 = *(const f32x4*)&ob[(((cs * 4 + ct) * 4 + q) * 64 + lane) * 4];
            f32x4 v;
#pragma unroll
            for (int e = 0; e < 4; ++e) v[e] = o[ct][4 * q + e] * a0 + o1[e] * a1;
            const int c4 = 32 * cs + 8 * ct + 2 * q + hf;
            *(u32x2*)(om + hh * 1024 + ((c4 ^ (hh & 15)) * 8)) = u32x2{cvtpk(v[0], v[1]), cvtpk(v[2], v[3])};
          }
        m = mn; l = l * a0 + v1[1] * a1;
      }
      LBAR();
#pragma unroll
      for (int i = 0; i < 4; ++i) {
        const int gi = i * 512 + tid, h2 = gi >> 6, c8 = gi & 63;
        const u32x2 v0 = *(const u32x2*)(om + h2 * 1024 + (((2 * c8) ^ (h2 & 15)) * 8)), v1 = *(const u32x2*)(om + h2 * 1024 + (((2 * c8 + 1) ^ (h2 & 15)) * 8));
        __builtin_amdgcn_raw_buffer_store_b128(u32x4{v0[0], v0[1], v1[0], v1[1]}, rs, WS_PAO + ((h2 * 2 + bA) * 32 + sp) * 1024 + c8 * 16, 0, 16);
      }
      asm volatile("s_waitcnt vmcnt(0)" ::: "memory");
      LBAR();
      if (wave == 0 && lane < 32) gst(rs, WS_PAML + ((hh * 2 + bA) * 32 + sp) * 16, __float_as_uint(m), __float_as_uint(l), ta, tbg);
    }
    SB();
    {
      const int ch = role & 1;
      u32x4 wv[8];
#pragma unroll
      for (int t = 0; t < 8; ++t) wv[t] = ldn(mkrs(Awuv, 1 << 22), ((256 * ch + 32 * wave + 8 * g) * 4096 + h * 128 + 8 * n) * 2, t * 8192);
      SB();
      const int b = wave >> 2, sq = wave & 3;
      const int mo = WS_PAML + ((h * 2 + b) * 32 + hh) * 16;
      u32x4 gm = gld(rs, mo);
      GWAIT(gm, mo, 16)
      u32x4 go[4];
#pragma unroll
      for (int s = 0; s < 4; ++s) go[s] = gld(rs, WS_PAO + ((h * 2 + b) * 32 + 8 * sq + 4 * hf + s) * 1024 + 512 * ch + hh * 16);
      const float ms = __uint_as_float(gm[0]), ls = __uint_as_float(gm[2]);
      const float M = x16_max(row_max(ms));
      float wt = ex2(ms - M);
      const float L = x16_sum(row_sum(wt * ls));
      wt = wt / L;
      f32x4 c0 = {0.f, 0.f, 0.f, 0.f}, c1 = c0;
#pragma unroll
      for (int s = 0; s < 4; ++s) { const float w_ = __shfl(wt, 8 * sq + 4 * hf + s);
        c0 += w_ * f32x4{bflo(go[s][0]), bfhi(go[s][0]), bflo(go[s][1]), bfhi(go[s][1])}; c1 += w_ * f32x4{bflo(go[s][2]), bfhi(go[s][2]), bflo(go[s][3]), bfhi(go[s][3])}; }
      float* cc = (float*)(lds + L_SX);
      *(f32x4*)&cc[((sq * 2 + hf) * 2 + b) * 256 + 8 * hh] = c0; *(f32x4*)&cc[((sq * 2 + hf) * 2 + b) * 256 + 8 * hh + 4] = c1;
      LBAR();
      FRESH_SEL(sel)
      u32 xh[4], xl[4];
#pragma unroll
      for (int k = 0; k < 2; ++k) {
        const int ci = (n >> 3) * 256 + 32 * wave + 8 * g + 4 * k;
        f32x4 v = *(const f32x4*)&cc[ci];
#pragma unroll
        for (int pp = 1; pp < 8; ++pp) v += *(const f32x4*)&cc[pp * 512 + ci];
        xh[2 * k] = cvtpk(v[0], v[1]); xh[2 * k + 1] = cvtpk(v[2], v[3]);
        xl[2 * k] = cvtpk(v[0] - bflo(xh[2 * k]), v[1] - bfhi(xh[2 * k])); xl[2 * k + 1] = cvtpk(v[2] - bflo(xh[2 * k + 1]), v[3] - bfhi(xh[2 * k + 1]));
      }
      f32x4 ca = {0.f, 0.f, 0.f, 0.f};
#pragma unroll
      for (int t = 0; t < 8; ++t) {
        ca = mfma16(place(xh[t >> 1], (t & 1) ? sel.o : sel.e), wv[t], ca);
        ca = mfma16(place(xl[t >> 1], (t & 1) ? sel.o : sel.e), wv[t], ca);
      }
      float* red = (float*)(lds + L_SX + 32768);
      *(f32x4*)&red[wave * 256 + (g >> 1) * 128 + 8 * n + 4 * (g & 1)] = ca;
      LBAR();
      if (wave >= 6) {
        const int pr = tid - 384;
        f32x2 s = {0.f, 0.f};
#pragma unroll
        for (int w = 0; w < 8; ++w) s += *(const f32x2*)&red[w * 256 + 2 * pr];
        gst(rs, WS_CTX + ((h * 2 + ch) * 128 + pr) * 16, __float_as_uint(s[0]), __float_as_uint(s[1]), ta, tbg);
      }
    }
    LBAR();
  }

  if (wave >= 4) {
    if (sw == 0) {
      const u32 coff = lane ? 0x7fffff00u : (u32)(WS_CNT + (h * 2 + bS) * 128);
      int prod = 0, left = 128 - T0;
      _Pragma("nounroll") for (u32 spin = 0; spin < (SPIN_CAP << 4); ++spin) {
        const int cns = (int)__builtin_amdgcn_readfirstlane(LDS_LD(&ctl[4]));
        const int want = cns + (left > BLEFT ? BAHD2 : BAHD) - bs;
        if (want > prod) {
          if (prod >= TQN - 8) { if (lane == 0) LDS_ST(&tq[prod], 128u); break; }
          int nc = left > CH4 ? 4 : left > CH2 ? 2 : 1;
          if (want - prod > nc) nc = want - prod < 4 ? want - prod : 4;
          const int base = T0 + __builtin_amdgcn_readfirstlane(__builtin_amdgcn_raw_ptr_buffer_atomic_add_i32(nc, rs, coff, 0, 0));
          if (lane < nc) LDS_ST(&tq[prod + lane], (u32)(base + lane));
          prod += nc; left = 128 - base - nc;
          if (left < 0) break;
        } else __builtin_amdgcn_s_sleep(4);
      }
      if (lane == 0) LDS_ST(&ctl[5], 1u);
    }
    if (!isA) return;
    u32* ob = (u32*)(lds + L_Q + 16384);
    if (sw == 1 || sw == 2) {
      const rsrc_t rw = mkrs((role == 6 ? Bwo : Awo) + (size_t)h * 65536, 131072);
      _Pragma("nounroll") for (int c = 0; c < 8; ++c) {
        u32x4 a[8];
        const int r0 = 16 * c + 8 * (sw - 1);
#pragma unroll
        for (int t = 0; t < 8; ++t) a[t] = ldn(rw, lane * 16, (r0 + t) * 1024);
#pragma unroll
        for (int t = 0; t < 8; ++t) { const int r = r0 + t; *(u32x4*)(lds + r * 1024 + (((lane >> 1) ^ ((r & 3) | ((r >> 1) & 4))) * 32) + (lane & 1) * 16) = a[t]; }
      }
      asm volatile("s_waitcnt lgkmcnt(0)" ::: "memory");
      if (lane == 0) __hip_atomic_fetch_add(&ctl[3], 1u, __ATOMIC_RELAXED, __HIP_MEMORY_SCOPE_WORKGROUP);
    } else if (sw == 3 && role == 7) {
#pragma unroll
      for (int k = 0; k < 2; ++k) {
        const int pr = 64 * k + lane, c0o = WS_CTX + ((h * 2) * 128 + pr) * 16;
        u32x4 g0 = gld(rs, c0o), g1 = gld(rs, c0o + 2048);
        GWAIT(g0, c0o, 8)
        GWAIT(g1, c0o + 2048, 8)
        ob[pr] = cvtpk(__uint_as_float(g0[0]) + __uint_as_float(g1[0]), __uint_as_float(g0[2]) + __uint_as_float(g1[2]));
      }
      asm volatile("s_waitcnt lgkmcnt(0)" ::: "memory");
      if (lane == 0) __hip_atomic_fetch_add(&ctl[6], 2u, __ATOMIC_RELAXED, __HIP_MEMORY_SCOPE_WORKGROUP);
    }
    CNT_WAIT(3, 2u, 8)
    u32x4 wf[8][4];
#pragma unroll
    for (int ct = 0; ct < 8; ++ct)
#pragma unroll
      for (int ks = 0; ks < 4; ++ks) {
        const int r = 32 * ks + 8 * g + (n >> 2), sz = (n >> 2) | ((g & 1) << 2);
        const u8* rp = lds + r * 1024 + (((8 * sw + ct) ^ sz) * 32) + 8 * (lane & 3);
        const u32x2 a = tr_read(rp), bq = tr_read(rp + 4096);
        wf[ct][ks] = u32x4{a[0], a[1], bq[0], bq[1]};
      }
    if (role == 6 && (sw == 1 || sw == 2)) {
      CNT_WAIT(5, 1u, 8)
      const int b = sw - 1;
      const int mo = WS_PBML + ((h * 2 + b) * 8 + (lane & 7)) * 16;
      u32x4 gm = z4, ga[4];
      u32 done = 0;
      float M = -3e30f; double L = 0.0, o0 = 0.0, o1 = 0.0;
      _Pragma("nounroll") for (u32 spin = 0; spin < SPIN_CAP; ++spin) {
        if (!((done >> ((lane & 7) >> 1)) & 1u)) gm = gld(rs, mo);
#pragma unroll
        for (int s = 0; s < 4; ++s) if (!((done >> s) & 1u)) ga[s] = gld(rs, WS_PB + (((h * 2 + b) * 4 + s) * 64 + lane) * 16);
        const u32 okm = (u32)__ballot(gok(gm, ta, tbg));
#define H1_INC(s_) if (!((done >> (s_)) & 1u) && ((okm >> (2 * (s_))) & 3u) == 3u && __all(ga[s_][3] == ta)) { done |= 1u << (s_); \
          const float ms_ = dppf<0x150 + 2 * (s_)>(__uint_as_float(gm[0])), Mn = fmaxf(M, ms_); const int eo = idiff(M, Mn), e_ = idiff(ms_, Mn); M = Mn; \
          L = __builtin_ldexp(L, eo) + __builtin_ldexp((double)dppf<0x150 + 2 * (s_)>(__uint_as_float(gm[2])) + (double)dppf<0x151 + 2 * (s_)>(__uint_as_float(gm[0])), e_); \
          o0 = __builtin_ldexp(o0, eo) + __builtin_ldexp((double)__uint_as_float(ga[s_][0]) + (double)bflo(ga[s_][2]), e_); \
          o1 = __builtin_ldexp(o1, eo) + __builtin_ldexp((double)__uint_as_float(ga[s_][1]) + (double)bfhi(ga[s_][2]), e_); }
        H1_INC(0) H1_INC(1) H1_INC(2) H1_INC(3)
        if (done == 15u) break;
        __builtin_amdgcn_s_sleep(TSLP);
      }
      const float iL = 1.f / (float)L;
      ob[b * 64 + lane] = cvtpk((float)o0 * iL, (float)o1 * iL);
      asm volatile("s_waitcnt lgkmcnt(0)" ::: "memory");
      if (lane == 0) __hip_atomic_fetch_add(&ctl[6], 1u, __ATOMIC_RELAXED, __HIP_MEMORY_SCOPE_WORKGROUP);
    }
    CNT_WAIT(6, 2u, 1)
    {
      u32x4 of[4];
#pragma unroll
      for (int ks = 0; ks < 4; ++ks) { const u32x4 v = *(const u32x4*)&ob[(n & 1) * 64 + 16 * ks + 4 * g]; of[ks] = n < 2 ? v : z4; }
      f32x4 y[8];
#pragma unroll
      for (int ct = 0; ct < 8; ++ct) y[ct] = f32x4{0.f, 0.f, 0.f, 0.f};
#pragma unroll
      for (int ks = 0; ks < 4; ++ks)
#pragma unroll
        for (int ct = 0; ct < 8; ++ct) y[ct] = mfma16(of[ks], wf[ct][ks], y[ct]);
      if (lane < 16) {
#pragma unroll
        for (int ct = 0; ct < 8; ++ct) gst(rs, (role == 6 ? WS_YB : WS_YA) + (((8 * sw + ct) * 32 + h) * 16 + n) * 16, __float_as_uint(y[ct][0]), __float_as_uint(y[ct][1]), ta, tbg);
      }
    }
    if (role == 7) {
      CNT_WAIT(5, 1u, 8)
      float* fr = (float*)(lds + L_Q + 17408);
#pragma unroll
      for (int pt = 0; pt < 2; ++pt) {
        const int yo = (pt ? WS_YB : WS_YA) + ((h * 32 + 8 * sw) * 16 + lane) * 16;
        u32x4 g0 = gld(rs, yo), g1 = gld(rs, yo + 1024);
        _Pragma("nounroll") for (u32 spin = 0; spin < SPIN_CAP; ++spin) {
          const bool k0 = gok(g0, ta, tbg), k1 = gok(g1, ta, tbg);
          if (__all(k0 && k1)) break;
          __builtin_amdgcn_s_sleep(TSLP);
          if (!k0) g0 = gld(rs, yo);
          if (!k1) g1 = gld(rs, yo + 1024);
        }
        const float s0 = x32_sum(x16_sum(__uint_as_float(g0[0]) + __uint_as_float(g1[0]))), s1 = x32_sum(x16_sum(__uint_as_float(g0[2]) + __uint_as_float(g1[2])));
        if (lane < 16) { fr[((sw * 2 + pt) * 2) * 16 + n] = s0; fr[((sw * 2 + pt) * 2 + 1) * 16 + n] = s1; }
      }
      CNT_BAR(7, 4u)
      if (sw == 0 && lane < 32) {
        f32x2 s = {0.f, 0.f};
#pragma unroll
        for (int w = 0; w < 4; ++w) s += *(const f32x2*)&fr[w * 64 + 2 * lane];
        *(u32*)&out[((lane >> 3) & 1) * 1024 + (lane >> 4) * 512 + 16 * h + 2 * (lane & 7)] = cvtpk(s[0], s[1]);
        if (h == 0 && lane == 0) __builtin_amdgcn_raw_buffer_store_b128(u32x4{enext, e1n, 0u, 0u}, rs, WS_EPOCH, 0, 16);
      }
    }
  } else {
    u32x4 U[W0T][8];
    if (!isA) {
#pragma unroll
      for (int hp = 0; hp < 2 * W0T; ++hp) {
        if (hp >= BPF) { USE4(U[(hp - BPF) >> 1][((hp - BPF) & 1) * 4 + 3]); SB(); }
        if (hp & 1) B_LV(U[hp >> 1], slot + 3 * (hp >> 1)) else B_LK(U[hp >> 1], slot + 3 * (hp >> 1))
        if (hp == 1) LBAR();
      }
    }
    CNT_WAIT(0, 8u, 2)
    u32x4 qf[4];
    { const u32x4 qv = *(const u32x4*)(lds + L_Q + 2048 + bS * 256 + 16 * (4 * g + (n & 3)));
#pragma unroll
      for (int t = 0; t < 4; ++t) qf[t] = (n >> 2) == t ? qv : z4; }
    u32 msk[4];
#pragma unroll
    for (int r = 0; r < 4; ++r) msk[r] = (n < 8 && (n >> 1) == r) ? ((n & 1) ? 0xffff0000u : 0x0000ffffu) : 0u;
    float m = -1e30f; double l = 0.0, acc[4] = {0.0, 0.0, 0.0, 0.0};
    SB();
#define B_QK(X_) f32x4 d = {0.f, 0.f, 0.f, 0.f}; _Pragma("unroll") for (int t = 0; t < 4; ++t) d = mfma16(X_[t], qf[t], d); asm volatile("" : "+v"(d) :: "memory"); SB();
#define B_PV1(X_, t_) { u32x4 a; const u32 pb = dppu<0x150 + 4 * (t_)>(p2); \
      a[0] = pb & msk[0]; a[1] = pb & msk[1]; a[2] = pb & msk[2]; a[3] = pb & msk[3]; tv = mfma16(a, X_[4 + (t_)], tv); }
#define B_SMPV(X_, valid_) { float sc = quad_diag_sum(d) * CS; if (!(valid_)) sc = -2e30f; \
      const float fl = __builtin_floorf(sc); \
      if (__any(fl >= m)) { const float mn = wave_max(fl) + 1.f; const int e_ = idiff(m, mn); m = mn; \
        l = __builtin_ldexp(l, e_); _Pragma("unroll") for (int r = 0; r < 4; ++r) acc[r] = __builtin_ldexp(acc[r], e_); } \
      const float p = __builtin_ldexpf(ex2(sc - fl), idiff(fl, m)); l += (double)p; const u32 p2 = cvtpk(p, p); \
      f32x4 tv = {0.f, 0.f, 0.f, 0.f}; \
      B_PV1(X_, 0) B_PV1(X_, 1) B_PV1(X_, 2) B_PV1(X_, 3) asm volatile("" : "+v"(tv) :: "memory"); SB(); \
      _Pragma("unroll") for (int r = 0; r < 4; ++r) acc[r] += (double)tv[r]; }
#define B_NEXT(tn_, kk_) u32 tn_ = (u32)(slot + 3 * (kk_)); \
      if (lane == 0) __hip_atomic_fetch_max(&ctl[4], (u32)(kk_), __ATOMIC_RELAXED, __HIP_MEMORY_SCOPE_WORKGROUP); \
      if ((kk_) >= bs) { \
        _Pragma("nounroll") for (u32 spin = 0; spin < (SPIN_CAP << 4); ++spin) { \
          tn_ = __builtin_amdgcn_readfirstlane(LDS_LD(&tq[(kk_) - bs])); \
          if (tn_ != 0xffffffffu) break; \
          __builtin_amdgcn_s_sleep(1); } \
        tn_ = tn_ < 128u ? tn_ : 128u; }
    const u32 ko2 = ko + wave * 4096, vo2 = vo + wave * 4096;
#define S_LK(X_, u_, t_) { const u32 so_ = (u32)(t_) * 16384u + (u_) * 4096u; _Pragma("unroll") for (int q_ = 0; q_ < 4; ++q_) X_[q_] = ldn(rk, ko2 + q_ * 1024, so_); SB(); }
#define S_LV(X_, u_, t_) { const u32 so_ = (u32)(t_) * 16384u + (u_) * 4096u; _Pragma("unroll") for (int q_ = 0; q_ < 4; ++q_) X_[4 + q_] = ldn(rv, vo2 + q_ * 1024, so_); SB(); }
#define S_STEP(tn_, valid_) { f32x4 d0, d1; \
      { B_QK(U[0]) d0 = d; } S_LK(U[0], 0, tn_) { B_QK(U[1]) d1 = d; } S_LK(U[1], 1, tn_) \
      { const f32x4 d = d0; B_SMPV(U[0], valid_) } S_LV(U[0], 0, tn_) { const f32x4 d = d1; B_SMPV(U[1], valid_) } S_LV(U[1], 1, tn_) }
    bool va = true;
    int k0 = W0T;
    if (!isA) {
      const u32 tf = wave < 2 ? (u32)(slot + 3 * W0T) : 128u;
      S_STEP(tf, true)
#pragma unroll
      for (int pp = 2; pp < W0T; ++pp) { B_QK(U[pp]) B_SMPV(U[pp], true) }
    } else {
      S_LK(U[0], 0, 128) S_LK(U[1], 1, 128) S_LV(U[0], 0, 128) S_LV(U[1], 1, 128)
      va = false; k0 = 0;
    }
    if (wave < 2) {
      _Pragma("nounroll") for (int k = k0; k < bs + TQN - 2; ++k) {
        B_NEXT(tn, k + 1)
        S_STEP(tn, va)
        va = tn < 128u;
        if (!va) break;
      }
    }
    float* pm = (float*)(lds + L_Q + 20480);
    double* pl = (double*)(lds + L_Q + 20480 + 64);
    double* pacc = (double*)(lds + L_Q + 21504);
    if (lane < 32) { *(f64x2*)&pacc[wave * 128 + 8 * n + 4 * g] = f64x2{acc[0], acc[1]}; *(f64x2*)&pacc[wave * 128 + 8 * n + 4 * g + 2] = f64x2{acc[2], acc[3]}; }
    l += dppd<0x124>(l); l += dppd<0x128>(l); l = x32_sumd(x16_sumd(l));
    if (lane == 0) { pm[wave] = m; pl[wave] = l; }
    asm volatile("s_waitcnt lgkmcnt(0)" ::: "memory");
    if (lane == 0) __hip_atomic_fetch_add(&ctl[2], 1u, __ATOMIC_RELAXED, __HIP_MEMORY_SCOPE_WORKGROUP);
    if (wave >= 2) return;
    CNT_WAIT(2, 4u, 1)
    if (wave == 0) {
      const f32x4 mv = *(const f32x4*)pm;
      const float M = fmaxf(fmaxf(mv[0], mv[1]), fmaxf(mv[2], mv[3]));
      double o0 = 0.0, o1 = 0.0;
#pragma unroll
      for (int w = 0; w < 4; ++w) { const int e = idiff(mv[w], M); const f64x2 a = *(const f64x2*)&pacc[w * 128 + 2 * lane]; o0 += __builtin_ldexp(a[0], e); o1 += __builtin_ldexp(a[1], e); }
      const float h0 = (float)o0, h1 = (float)o1;
      __builtin_amdgcn_raw_buffer_store_b128(u32x4{__float_as_uint(h0), __float_as_uint(h1), cvtpk((float)(o0 - (double)h0), (float)(o1 - (double)h1)), ta},
                                             rs, WS_PB + (((h * 2 + bS) * 4 + slot) * 64 + lane) * 16, 0, 16);
    } else {
      const f32x4 mv = *(const f32x4*)pm;
      const float M = fmaxf(fmaxf(mv[0], mv[1]), fmaxf(mv[2], mv[3]));
      double L = 0.0;
#pragma unroll
      for (int w = 0; w < 4; ++w) L += __builtin_ldexp(pl[w], idiff(mv[w], M));
      const float Lh = (float)L;
      if (lane < 2) gst(rs, WS_PBML + (((h * 2 + bS) * 4 + slot) * 2 + lane) * 16, __float_as_uint(lane ? (float)(L - (double)Lh) : M), __float_as_uint(lane ? 0.f : Lh), ta, tbg);
    }
  }

}

extern "C" void kernel_launch(void* const* d_in, const int* in_sizes, int n_in, void* d_out, int out_size, void* d_ws, size_t ws_size, hipStream_t stream) {
  constexpr int LDSB = 163840;
  (void)hipFuncSetAttribute((const void*)fused, hipFuncAttributeMaxDynamicSharedMemorySize, LDSB);
  fused<<<256, 512, LDSB, stream>>>((const u16*)d_in[0], (const u16*)d_in[1], (const u16*)d_in[2], (const u16*)d_in[3], (const u16*)d_in[4], (const u16*)d_in[5],
                                    (const u16*)d_in[6], (const u16*)d_in[7], (const u16*)d_in[8], (const u16*)d_in[9], (const u16*)d_in[10], (u16*)d_out, (unsigned char*)d_ws);
}
```
